```python
import torch

def _softmax(x, axis=-1):
    return torch.softmax(x, dim=axis)
def _gelu(x):
    return torch.nn.functional.gelu(x, approximate="tanh")
def _silu(x):
    return torch.nn.functional.silu(x)
def _sigmoid(x):
    return torch.sigmoid(x)
def _layernorm(x, axis=-1, eps=1e-5):
    m = x.mean(dim=axis, keepdim=True)
    v = x.var(dim=axis, keepdim=True, unbiased=False)
    return (x - m) / torch.sqrt(v + eps)
def _scatter_add(tgt, idx, src, axis=0):
    return tgt.clone().index_add_(axis, idx, src)
def _segment_sum(src, seg, n):
    out = torch.zeros((n,) + src.shape[1:], dtype=src.dtype)
    return out.index_add_(0, seg, src)
def _pad(x, pw):
    flat = tuple(v for pair in reversed(pw) for v in pair)
    return torch.nn.functional.pad(x, flat)

kernel_name = 'gated_mix__sliding_block__dense_activation_relu__prompt'

def setup_inputs():

    _w = (lambda a: (lambda t: t.to(torch.bfloat16) if t.is_floating_point() else t)(torch.as_tensor(a)))
    def _A_setup():
        B, S, D = 1, 2048, 512
        return {
            "x": _w((lambda *s: torch.randn(*s, dtype=torch.bfloat16) * 0.05)(B, S, D)),
            "wqkv": _w((lambda *s: torch.randn(*s, dtype=torch.bfloat16) * 0.8)(D, 3 * D)),
            "wo": _w((lambda *s: torch.randn(*s, dtype=torch.bfloat16) * 0.05)(D, D)),
        }
    _A_in = _A_setup()

    def _B_setup():
        B, S, D, Dff = 1, 2048, 512, 1376
        return {
            "x": _w((lambda *s: torch.randn(*s, dtype=torch.bfloat16) * 0.05)(B, S, D)),
            "w1": _w((lambda *s: torch.randn(*s, dtype=torch.bfloat16) * 0.05)(D, Dff)),
            "w2": _w((lambda *s: torch.randn(*s, dtype=torch.bfloat16) * 0.05)(Dff, D)),
        }
    _B_in = _B_setup()

    return {
        "A_x": _A_in["x"],
        "A_wqkv": _A_in["wqkv"],
        "A_wo": _A_in["wo"],
        "B_x": _B_in["x"],
        "B_w1": _B_in["w1"],
        "B_w2": _B_in["w2"],
        "gate_w": _w((lambda *s: torch.randn(*s, dtype=torch.bfloat16) * 0.05)(512) * 16.0),
    }

def reference(A_x, A_wqkv, A_wo, B_x, B_w1, B_w2, gate_w):
    def _A_ref(x, wqkv, wo):
        B, S, D = x.shape
        H = 32; Dh = D // H; W = 256
        qkv = (lambda x, *d: x.permute(*d))((x @ wqkv).reshape(B, S, 3, H, Dh), 2, 0, 3, 1, 4)
        q, k, v = qkv[0], qkv[1], qkv[2]
        scores = torch.einsum("bhqd,bhkd->bhqk", q, k) * (Dh ** -0.5)
        idx = torch.arange(S)
        mask = (idx[:, None] - idx[None, :] >= 0) & (idx[:, None] - idx[None, :] < W)
        scores = torch.where(mask[None, None], scores, -1e9)
        p = _softmax(scores, axis=-1)
        o = torch.einsum("bhqk,bhkd->bhqd", p, v)
        return (lambda x, *d: x.permute(*d))(o, 0, 2, 1, 3).reshape(B, S, D) @ wo

    def _B_ref(x, w1, w2):
        h = torch.einsum("bsd,df->bsf", x, w1)
        zero = torch.zeros_like(h)
        h = torch.maximum(h, zero)
        return torch.einsum("bsf,fd->bsd", h, w2)

    x = A_x
    g = _sigmoid((x * gate_w).sum(-1, keepdims=True))
    a = _A_ref(x, A_wqkv, A_wo)
    b = _B_ref(x, B_w1, B_w2)
    return g * a + (1 - g) * b
```

```cpp
#include <hip/hip_runtime.h>
#include <hip/hip_bf16.h>
#include <stdint.h>

#define DI __device__ __forceinline__
#define LAS __attribute__((address_space(3)))
#define GAS __attribute__((address_space(1)))
typedef short bf16x8 __attribute__((ext_vector_type(8)));
typedef short s16x4 __attribute__((ext_vector_type(4)));
typedef float f32x16 __attribute__((ext_vector_type(16)));
typedef float f32x4 __attribute__((ext_vector_type(4)));
typedef float f32x2 __attribute__((ext_vector_type(2)));
typedef __bf16 bf16x2_t __attribute__((ext_vector_type(2)));
typedef unsigned u32x4 __attribute__((ext_vector_type(4)));
typedef unsigned u32x2 __attribute__((ext_vector_type(2)));

constexpr int S_ = 2048, D_ = 512, DFF = 1376;
constexpr int NKS_H = DFF / 16;
constexpr int NKS = NKS_H + 32;
constexpr size_t WS_Q = 0;
constexpr size_t WS_K = WS_Q + (2u << 20);
constexpr size_t WS_V = WS_K + (2u << 20);
constexpr size_t WS_A = WS_V + (2u << 20);
constexpr size_t WS_G = WS_A + (size_t)64 * NKS * 1024;
constexpr size_t WS_END = WS_G + 8192;

constexpr float QSCALE = 0.25f * 1.4426950408889634f;

#define MFMA32(a, b, c) __builtin_amdgcn_mfma_f32_32x32x16_bf16((a), (b), (c), 0, 0, 0)

DI unsigned cvtpk(float lo, float hi) { f32x2 v = {lo, hi}; bf16x2_t b = __builtin_convertvector(v, bf16x2_t); return __builtin_bit_cast(unsigned, b); }
DI u32x4 pack8(const f32x16& x, int s, float sc) {
    u32x4 p;
    p[0] = cvtpk(x[8 * s + 0] * sc, x[8 * s + 1] * sc); p[1] = cvtpk(x[8 * s + 2] * sc, x[8 * s + 3] * sc);
    p[2] = cvtpk(x[8 * s + 4] * sc, x[8 * s + 5] * sc); p[3] = cvtpk(x[8 * s + 6] * sc, x[8 * s + 7] * sc);
    return p;
}
DI void dma16(unsigned lds_dst, unsigned voff, __amdgpu_buffer_rsrc_t rs, unsigned soff) {
    asm volatile("s_mov_b32 m0, %0\n\ts_nop 0\n\tbuffer_load_dwordx4 %1, %2, %3 offen lds" ::"s"(lds_dst), "v"(voff), "s"(rs), "s"(soff) : "memory");
}
#define WAITV_BAR(n) asm volatile("s_waitcnt vmcnt(" #n ") lgkmcnt(0)\n\ts_barrier" ::: "memory")
#define BAR_L() asm volatile("s_waitcnt lgkmcnt(0)\n\ts_barrier" ::: "memory")
DI s16x4 trd(const LAS char* p) { return __builtin_bit_cast(s16x4, __builtin_amdgcn_ds_read_tr16_b64_v4i16((LAS s16x4*)p)); }
DI bf16x8 cat8(s16x4 lo, s16x4 hi) { return __builtin_shufflevector(lo, hi, 0, 1, 2, 3, 4, 5, 6, 7); }
DI __amdgpu_buffer_rsrc_t mkrs(const void* p, unsigned bytes) { return __builtin_amdgcn_make_buffer_rsrc((void*)p, 0, (int)bytes, 0x00020000); }

DI void block_map(int b, int& rb, int& hp) { const int xg = b & 7, idx = b >> 3; rb = (xg >> 2) * 8 + (idx >> 2); hp = (xg & 3) * 4 + (idx & 3); }

constexpr int L_XS1 = 0, L_XS0 = 32768, L_RED = 32768, L_MISC = 81920, L_WR = 86016;
constexpr int L_GW = L_MISC, L_GP = L_MISC + 1024;
constexpr int L_WSLOT = 24576;
constexpr int P1_LDS = L_WR + 3 * L_WSLOT;
static_assert(P1_LDS <= 163840, "lds");

__global__ __launch_bounds__(512, 2) void k_phase1(const void* x, const void* wqkv, const void* w1, const void* gw, char* ws) {
    extern __shared__ __attribute__((aligned(16))) char smem[];
    LAS char* L = (LAS char*)smem;
    const int tid = threadIdx.x, lane = tid & 63;
    int w = __builtin_amdgcn_readfirstlane(tid >> 6);
    asm volatile("" : "+s"(w));
    const int m = w >> 1, kh = w & 1, r = lane & 31, h = lane >> 5;
    int rb, hp; block_map(blockIdx.x, rb, hp);
    const unsigned ldsb = (unsigned)(uintptr_t)L;
    const __amdgpu_buffer_rsrc_t rx = mkrs(x, S_ * D_ * 2), rq = mkrs(wqkv, D_ * 3 * D_ * 2), r1 = mkrs(w1, D_ * DFF * 2), rg = mkrs(gw, D_ * 2);
    unsigned voX;
    { const int row = 8 * w + (lane >> 3), pos = lane & 7, c = pos ^ ((row >> 1) & 7); voX = (unsigned)(row * 1024 + c * 16); }
    unsigned voQ[3], vo1[3];
    const int u0 = (hp * 3 < 40) ? hp * 3 : 40;
#pragma unroll
    for (int t = 0; t < 3; ++t) {
        const int li = 64 * (w + 8 * t) + lane, row = li / 12, c = li - row * 12;
        voQ[t] = (unsigned)(row * 3072 + (c >> 2) * 1024 + hp * 64 + (c & 3) * 16);
        vo1[t] = (unsigned)(row * 2752 + u0 * 64 + c * 16);
    }
    const unsigned sX = (unsigned)rb * 128u * 1024u;
    auto issueX = [&](int sc) {
        const unsigned dst = ldsb + ((sc & 1) ? L_XS1 : L_XS0) + (unsigned)w * 1024u;
#pragma unroll
        for (int t = 0; t < 4; ++t) dma16(dst + (unsigned)t * 8192u, voX, rx, sX + (unsigned)sc * 256u + (unsigned)(t & 1) * 65536u + (unsigned)(t >> 1) * 128u);
    };
    auto issueW = [&](int i) {
        const unsigned dst = ldsb + L_WR + (unsigned)(i % 3) * L_WSLOT + (unsigned)w * 1024u;
#pragma unroll
        for (int t = 0; t < 3; ++t) {
            if (i < 4) dma16(dst + (unsigned)t * 8192u, voQ[t], rq, (unsigned)i * 128u * 3072u);
            else dma16(dst + (unsigned)t * 8192u, vo1[t], r1, (unsigned)(i - 4) * 128u * 2752u);
        }
    };
    if (lane < 8) dma16(ldsb + L_GW + (unsigned)w * 128u, (unsigned)(w * 128 + lane * 16), rg, 0u);
    issueX(0); issueW(0); issueX(1); issueW(1); issueW(2);

    const int xrow = m * 32 + r;
    unsigned xoff[2];
#pragma unroll
    for (int s = 0; s < 2; ++s) { const int c = (2 * kh + s) * 2 + h; xoff[s] = (unsigned)(xrow * 128 + ((c ^ ((xrow >> 1) & 7)) * 16)); }
    const int blk = (lane >> 4) & 1;
    const unsigned wrow = (unsigned)((8 * h + ((lane & 15) >> 2)) * 192 + (lane & 3) * 8 + kh * 32 * 192);
    unsigned woff[3];
    woff[0] = wrow + (kh ? 128u : 0u) + blk * 32; woff[1] = wrow + 64u + (blk ^ kh) * 32; woff[2] = wrow + (kh ? 0u : 128u) + blk * 32;

    bf16x8 xf[16];
    f32x16 acc0, acc1, acc2;
#pragma unroll
    for (int j = 0; j < 16; ++j) { acc0[j] = 0.f; acc1[j] = 0.f; acc2[j] = 0.f; }
    float gsum = 0.f;

    auto readX = [&](int sc) {
        const LAS char* base = L + ((sc & 1) ? L_XS1 : L_XS0);
#pragma unroll
        for (int c = 0; c < 2; ++c)
#pragma unroll
            for (int s = 0; s < 2; ++s) xf[sc * 4 + c * 2 + s] = *(const LAS bf16x8*)(base + c * 16384 + xoff[s]);
    };
    auto gate = [&](int sc) {
#pragma unroll
        for (int c = 0; c < 2; ++c)
#pragma unroll
            for (int s = 0; s < 2; ++s) {
                const bf16x8 gwf = *(const LAS bf16x8*)(L + L_GW + (sc * 128 + c * 64 + (2 * kh + s) * 16 + 8 * h) * 2);
                const u32x4 a = __builtin_bit_cast(u32x4, xf[sc * 4 + c * 2 + s]), b = __builtin_bit_cast(u32x4, gwf);
#pragma unroll
                for (int j = 0; j < 4; ++j) { const unsigned aj = a[j], bj = b[j]; gsum = __builtin_amdgcn_fdot2_f32_bf16(__builtin_bit_cast(bf16x2_t, aj), __builtin_bit_cast(bf16x2_t, bj), gsum, false); }
            }
    };
    auto compute = [&](int i) {
        const LAS char* wb = L + L_WR + (i % 3) * L_WSLOT;
        const int sc = i & 3;
#pragma unroll
        for (int c = 0; c < 2; ++c)
#pragma unroll
            for (int s = 0; s < 2; ++s) {
                const int krow = (c * 64 + s * 16) * 192;
                const bf16x8 xx = xf[sc * 4 + c * 2 + s];
                acc0 = MFMA32(cat8(trd(wb + woff[0] + krow), trd(wb + woff[0] + krow + 4 * 192)), xx, acc0);
                acc1 = MFMA32(cat8(trd(wb + woff[1] + krow), trd(wb + woff[1] + krow + 4 * 192)), xx, acc1);
                acc2 = MFMA32(cat8(trd(wb + woff[2] + krow), trd(wb + woff[2] + krow + 4 * 192)), xx, acc2);
            }
    };
    auto reduce = [&]() {
        LAS f32x4* red = (LAS f32x4*)(L + L_RED);
        LAS f32x4* dst = red + w * 6 * 64 + lane;
        const LAS f32x4* src = red + (w ^ 1) * 6 * 64 + lane;
#pragma unroll
        for (int q = 0; q < 4; ++q) dst[q * 64] = (f32x4){acc2[4 * q], acc2[4 * q + 1], acc2[4 * q + 2], acc2[4 * q + 3]};
#pragma unroll
        for (int q = 0; q < 2; ++q) dst[(4 + q) * 64] = (f32x4){acc1[8 + 4 * q], acc1[8 + 4 * q + 1], acc1[8 + 4 * q + 2], acc1[8 + 4 * q + 3]};
        BAR_L();
#pragma unroll
        for (int q = 0; q < 4; ++q) { const f32x4 v = src[q * 64];
#pragma unroll
            for (int j = 0; j < 4; ++j) acc0[4 * q + j] += v[j]; }
#pragma unroll
        for (int q = 0; q < 2; ++q) { const f32x4 v = src[(4 + q) * 64];
#pragma unroll
            for (int j = 0; j < 4; ++j) acc1[4 * q + j] += v[j]; }
    };

    WAITV_BAR(10); readX(0); BAR_L(); issueX(2); gate(0); compute(0);
    WAITV_BAR(7); issueW(3); readX(1); BAR_L(); issueX(3); gate(1); compute(1);
    WAITV_BAR(7); issueW(4); readX(2); gate(2); compute(2);
    WAITV_BAR(3); issueW(5); readX(3); gate(3); compute(3);
    {
        float g2 = gsum + __shfl_xor(gsum, 32);
        if (h == 0) ((LAS float*)(L + L_GP))[kh * 128 + xrow] = g2;
    }
    reduce();
    const float gz = ((const LAS float*)(L + L_GP))[xrow] + ((const LAS float*)(L + L_GP))[128 + xrow];
    const float g = 1.f / (1.f + __expf(-gz));
    const int tb = rb * 4 + m;
    {
        GAS u32x4* QV = (GAS u32x4*)(ws + (kh ? WS_V : WS_Q)), *K = (GAS u32x4*)(ws + WS_K);
        const float sc = kh ? 1.f : QSCALE;
        const int ql = kh ? (2 * r + h) : lane;
        QV[((size_t)(hp * 64 + tb) * 2 + 0) * 64 + ql] = pack8(acc0, 0, sc);
        QV[((size_t)(hp * 64 + tb) * 2 + 1) * 64 + ql] = pack8(acc0, 1, sc);
        K[((size_t)(hp * 64 + tb) * 2 + kh) * 64 + lane] = pack8(acc1, 0, 1.f);
        if (h == 0 && hp == 0 && kh == 0) ((GAS float*)(ws + WS_G))[rb * 128 + xrow] = g;
    }
#pragma unroll
    for (int j = 0; j < 16; ++j) { acc0[j] = 0.f; acc1[j] = 0.f; acc2[j] = 0.f; }
    if (hp * 3 < 43) {
        WAITV_BAR(3); issueW(6); compute(4);
        WAITV_BAR(3); issueW(7); compute(5);
        WAITV_BAR(3); compute(6);
        WAITV_BAR(0); compute(7);
        reduce();
        const float og = 1.f - g;
#pragma unroll
        for (int j = 0; j < 16; ++j) { acc0[j] = fmaxf(acc0[j], 0.f); acc1[j] = fmaxf(acc1[j], 0.f); }
        GAS u32x4* A = (GAS u32x4*)(ws + WS_A) + ((size_t)tb * NKS + 2 * u0) * 64 + lane;
        A[(kh ? 4 : 0) * 64] = pack8(acc0, 0, og);
        A[(kh ? 5 : 1) * 64] = pack8(acc0, 1, og);
        A[(kh ? 3 : 2) * 64] = pack8(acc1, 0, og);
    } else {
        asm volatile("s_waitcnt vmcnt(0)" ::: "memory");
    }
}

constexpr int A_K = 0, A_V = 24576, A_Q = 49152, A_ONE = 57344, P2_LDS = 57344 + 256;
__global__ __launch_bounds__(512, 2) void k_attn(char* ws) {
    extern __shared__ __attribute__((aligned(16))) char smem[];
    LAS char* L = (LAS char*)smem;
    const int tid = threadIdx.x, lane = tid & 63;
    int w = __builtin_amdgcn_readfirstlane(tid >> 6);
    asm volatile("" : "+s"(w));
    const int qb = w >> 1, hd = w & 1, r = lane & 31, h = lane >> 5;
    int rb, hp; block_map(blockIdx.x, rb, hp);
    const int tb = rb * 4 + qb;
    const unsigned ldsb = (unsigned)(uintptr_t)L;
    const __amdgpu_buffer_rsrc_t rw = mkrs(ws, (unsigned)WS_A);
#pragma unroll
    for (int t = 0; t < 3; ++t) {
        const int pc = w + 8 * t, i = pc >> 1, hh = pc & 1;
        const int kt = rb * 4 - 8 + i, ktc = kt < 0 ? 0 : kt;
        const unsigned src = (unsigned)(((hp * 64 + ktc) * 2 + hh) * 1024);
        dma16(ldsb + A_K + (unsigned)(hh * 12 + i) * 1024u, (unsigned)lane * 16u, rw, (unsigned)WS_K + src);
        dma16(ldsb + A_V + (unsigned)(hh * 12 + i) * 1024u, (unsigned)lane * 16u, rw, (unsigned)WS_V + src);
    }
    dma16(ldsb + A_Q + (unsigned)w * 1024u, (unsigned)lane * 16u, rw, (unsigned)WS_Q + (unsigned)(((hp * 64 + tb) * 2 + hd) * 1024));
    if (tid < 64) ((LAS unsigned*)(L + A_ONE))[tid] = 0x3f803f80u;
    const float g = ((const GAS float*)(ws + WS_G))[tb * 32 + r];
    WAITV_BAR(0);
    const bf16x8 qf = *(const LAS bf16x8*)(L + A_Q + w * 1024 + lane * 16);
    const int t0 = tb >= 8 ? 0 : 8 - tb;
    const LAS char* kb = L + A_K + (hd * 12 + qb) * 1024 + lane * 16;
    const int blk = (lane >> 4) & 1;
    const LAS char* vb = blk ? (L + A_ONE + (1 - h) * 128) : (L + A_V + (hd * 12 + qb) * 1024 + (4 * h + ((lane & 15) >> 2)) * 32 + (lane & 3) * 8);
    const int vstep = blk ? 0 : 1;
    bf16x8 kf[9];
#pragma unroll
    for (int i = 0; i < 9; ++i) kf[i] = *(const LAS bf16x8*)(kb + i * 1024);
    f32x16 zero;
#pragma unroll
    for (int j = 0; j < 16; ++j) zero[j] = 0.f;
    const float NEG = -INFINITY;
    float mx = NEG;
#pragma unroll
    for (int i = 0; i < 9; ++i) {
        if (i >= t0) {
            f32x16 s = MFMA32(kf[i], qf, zero);
#pragma unroll
            for (int j = 0; j < 16; ++j) {
                const int key = (j & 3) + 8 * (j >> 2) + 4 * h;
                float v = s[j];
                if (i == 0) v = key > r ? v : NEG;
                if (i == 8) v = key <= r ? v : NEG;
                mx = fmaxf(mx, v);
            }
        }
    }
    mx = fmaxf(mx, __shfl_xor(mx, 32));
    f32x16 negm;
#pragma unroll
    for (int j = 0; j < 16; ++j) negm[j] = -mx;
    f32x16 o = zero;
#pragma unroll
    for (int i = 0; i < 9; ++i) {
        if (i >= t0) {
            f32x16 s = MFMA32(kf[i], qf, negm);
#pragma unroll
            for (int j = 0; j < 16; ++j) {
                const int key = (j & 3) + 8 * (j >> 2) + 4 * h;
                float p = __builtin_amdgcn_exp2f(s[j]);
                if (i == 0) p = key > r ? p : 0.f;
                if (i == 8) p = key <= r ? p : 0.f;
                s[j] = p;
            }
#pragma unroll
            for (int ss = 0; ss < 2; ++ss) {
                const LAS char* p0 = vb + vstep * (i * 1024 + ss * 512);
                const bf16x8 vf = cat8(trd(p0), trd(p0 + vstep * 256));
                o = MFMA32(vf, __builtin_bit_cast(bf16x8, pack8(s, ss, 1.f)), o);
            }
        }
    }
    const float sc = g / o[8];
    ((GAS u32x4*)(ws + WS_A))[((size_t)tb * NKS + NKS_H + hp * 2 + hd) * 64 + lane] = pack8(o, 0, sc);
}

constexpr int P3_NSLOT = 12;
constexpr int P3_LDS = 163840;
constexpr int NCH = 30;
__global__ __launch_bounds__(512, 2) void k_phase3(const void* w2, const void* wo, char* ws, void* out) {
    extern __shared__ __attribute__((aligned(16))) char smem[];
    LAS char* L = (LAS char*)smem;
    const int tid = threadIdx.x, lane = tid & 63;
    int w = __builtin_amdgcn_readfirstlane(tid >> 6);
    asm volatile("" : "+s"(w));
    const int m = w & 1, kq = w >> 1, r = lane & 31, h = lane >> 5;
    int rb, j16; block_map(blockIdx.x, rb, j16);
    const int rh = j16 >> 3, ct = j16 & 7;
    const unsigned ldsb = (unsigned)(uintptr_t)L;
    const __amdgpu_buffer_rsrc_t r2 = mkrs(w2, DFF * D_ * 2), ro = mkrs(wo, D_ * D_ * 2);
    unsigned voW, voO;
    { const int row = lane >> 3, pos = lane & 7, c = pos ^ (((row >> 1) & 1) << 2); voW = (unsigned)(row * 1024 + ct * 128 + c * 16);
      const int a = w & 1, hh = row >> 2, q = row & 3;
      voO = (unsigned)((8 * hh + 4 * a + q) * 1024 + ct * 128 + c * 16); }
    auto issueW = [&](int c) {
        const int row0 = 64 * c + 8 * w;
        const unsigned dst = ldsb + (unsigned)(c % P3_NSLOT) * 8192u + (unsigned)w * 1024u;
        if (row0 < DFF) dma16(dst, voW, r2, (unsigned)row0 * 1024u);
        else if (row0 < DFF + D_) dma16(dst, voO, ro, (unsigned)((row0 - DFF) & ~15) * 1024u);
        else dma16(dst, voW, ro, 0u);
    };
    const int tb = rb * 4 + rh * 2 + m;
    const GAS bf16x8* A = (const GAS bf16x8*)(ws + WS_A) + (size_t)tb * NKS * 64 + lane;
#pragma unroll
    for (int c = 0; c < P3_NSLOT; ++c) issueW(c);
    bf16x8 af[NCH];
#pragma unroll
    for (int c = 0; c < NCH; ++c) { const int ks = 4 * c + kq; af[c] = A[(size_t)(ks < NKS ? ks : 0) * 64]; }
    const int q = (lane & 15) >> 2, p = lane & 3, blk = (lane >> 4) & 1;
    unsigned woff[2];
#pragma unroll
    for (int t = 0; t < 2; ++t) { const int row = 16 * kq + 4 * h + q; const int c16 = (t * 4 + blk * 2 + (p >> 1)) ^ (((row >> 1) & 1) << 2); woff[t] = (unsigned)(row * 128 + c16 * 16 + (p & 1) * 8); }
    f32x16 acc[2];
#pragma unroll
    for (int i = 0; i < 2; ++i)
#pragma unroll
        for (int j = 0; j < 16; ++j) acc[i][j] = 0.f;
#pragma unroll
    for (int c = 0; c < NCH; ++c) {
        asm volatile("s_waitcnt vmcnt(0) lgkmcnt(0)\n\ts_barrier" ::: "memory");
        if (c >= 1 && c - 1 + P3_NSLOT < NCH) issueW(c - 1 + P3_NSLOT);
        if (4 * c + kq < NKS) {
            const LAS char* wb = L + (c % P3_NSLOT) * 8192;
#pragma unroll
            for (int t = 0; t < 2; ++t) {
                const bf16x8 wf = cat8(trd(wb + woff[t]), trd(wb + woff[t] + 8 * 128));
                acc[t] = MFMA32(wf, af[c], acc[t]);
            }
        }
    }
    BAR_L();
    constexpr int RP = 272;
    {
        LAS char* dst = L + kq * (64 * RP) + (m * 32 + r) * RP;
#pragma unroll
        for (int t = 0; t < 2; ++t)
#pragma unroll
            for (int g4 = 0; g4 < 4; ++g4)
                *(LAS f32x4*)(dst + (t * 32 + g4 * 8 + 4 * h) * 4) = (f32x4){acc[t][4 * g4], acc[t][4 * g4 + 1], acc[t][4 * g4 + 2], acc[t][4 * g4 + 3]};
    }
    BAR_L();
    {
        const int tok = tid >> 3, oc = tid & 7;
        f32x4 a = {0.f, 0.f, 0.f, 0.f}, b = a;
#pragma unroll
        for (int k = 0; k < 4; ++k) {
            const LAS char* src = L + k * (64 * RP) + tok * RP + oc * 32;
            a += *(const LAS f32x4*)src; b += *(const LAS f32x4*)(src + 16);
        }
        u32x4 o = {cvtpk(a[0], a[1]), cvtpk(a[2], a[3]), cvtpk(b[0], b[1]), cvtpk(b[2], b[3])};
        *(GAS u32x4*)((char*)out + ((size_t)(rb * 128 + rh * 64 + tok) * 512 + ct * 64 + oc * 8) * 2) = o;
    }
}

extern "C" void kernel_launch(void* const* d_in, const int* in_sizes, int n_in, void* d_out, int out_size, void* d_ws, size_t ws_size, hipStream_t stream) {
    (void)hipFuncSetAttribute((const void*)k_phase1, hipFuncAttributeMaxDynamicSharedMemorySize, P1_LDS);
    (void)hipFuncSetAttribute((const void*)k_phase3, hipFuncAttributeMaxDynamicSharedMemorySize, P3_LDS);
    k_phase1<<<256, 512, P1_LDS, stream>>>(d_in[0], d_in[1], d_in[4], d_in[6], (char*)d_ws);
    k_attn<<<256, 512, P2_LDS, stream>>>((char*)d_ws);
    k_phase3<<<256, 512, P3_LDS, stream>>>(d_in[5], d_in[2], (char*)d_ws, d_out);
}
```

```cpp
#include <hip/hip_runtime.h>
#include <hip/hip_bf16.h>
#include <stdint.h>

#define DI __device__ __forceinline__
#define LAS __attribute__((address_space(3)))
#define GAS __attribute__((address_space(1)))
typedef short bf16x8 __attribute__((ext_vector_type(8)));
typedef short s16x4 __attribute__((ext_vector_type(4)));
typedef float f32x16 __attribute__((ext_vector_type(16)));
typedef float f32x4 __attribute__((ext_vector_type(4)));
typedef float f32x2 __attribute__((ext_vector_type(2)));
typedef __bf16 bf16x2_t __attribute__((ext_vector_type(2)));
typedef unsigned u32x4 __attribute__((ext_vector_type(4)));
typedef unsigned u32x2 __attribute__((ext_vector_type(2)));
typedef __amdgpu_buffer_rsrc_t rsrc_t;

constexpr int S_ = 2048, D_ = 512, DFF = 1376;
constexpr int NKS_H = DFF / 16;
constexpr int NKS = NKS_H + 32;
constexpr unsigned WS_E = 0;
constexpr unsigned WS_FC = 4096;
constexpr unsigned WS_FH = 8192;
constexpr unsigned WS_K = 1u << 20;
constexpr unsigned WS_V = WS_K + (2u << 20);
constexpr unsigned WS_A = WS_V + (2u << 20);
constexpr unsigned WS_END = WS_A + 64u * NKS * 1024u;
constexpr unsigned WS_DBG = WS_END;
constexpr unsigned SENT = 0xFFFFFFFFu;
constexpr unsigned SPIN_CAP = 1u << 18;

constexpr float QSCALE = 0.25f * 1.4426950408889634f;

#define MFMA32(a, b, c) __builtin_amdgcn_mfma_f32_32x32x16_bf16((a), (b), (c), 0, 0, 0)

DI unsigned cvtpk(float lo, float hi) { f32x2 v = {lo, hi}; bf16x2_t b = __builtin_convertvector(v, bf16x2_t); return __builtin_bit_cast(unsigned, b); }
DI u32x4 pack8(const f32x16& x, int s, float sc) {
    u32x4 p;
    p[0] = cvtpk(x[8 * s + 0] * sc, x[8 * s + 1] * sc); p[1] = cvtpk(x[8 * s + 2] * sc, x[8 * s + 3] * sc);
    p[2] = cvtpk(x[8 * s + 4] * sc, x[8 * s + 5] * sc); p[3] = cvtpk(x[8 * s + 6] * sc, x[8 * s + 7] * sc);
    return p;
}
DI u32x4 nosent(u32x4 p) {
#pragma unroll
    for (int j = 0; j < 4; ++j) { const unsigned v = p[j]; p[j] = v == SENT ? 0x7FC07FC0u : v; }
    return p;
}
DI void dma16(unsigned lds_dst, unsigned voff, rsrc_t rs, unsigned soff) {
    asm volatile("s_mov_b32 m0, %0\n\ts_nop 0\n\tbuffer_load_dwordx4 %1, %2, %3 offen lds" ::"s"(lds_dst), "v"(voff), "s"(rs), "s"(soff) : "memory");
}
DI void ld16c(u32x4& v, unsigned voff, rsrc_t rs, unsigned soff) {
    asm volatile("buffer_load_dwordx4 %0, %1, %2, %3 offen sc1" : "=&v"(v) : "v"(voff), "s"(rs), "s"(soff) : "memory");
}
DI void ld8c(u32x2& v, unsigned voff, rsrc_t rs, unsigned soff) {
    asm volatile("buffer_load_dwordx2 %0, %1, %2, %3 offen sc1" : "=&v"(v) : "v"(voff), "s"(rs), "s"(soff) : "memory");
}
DI void st16c(u32x4 v, unsigned voff, rsrc_t rs, unsigned soff) {
    asm volatile("buffer_store_dwordx4 %0, %1, %2, %3 offen sc1\n\ts_nop 1" ::"v"(v), "v"(voff), "s"(rs), "s"(soff) : "memory");
}
DI void st8c(u32x2 v, unsigned voff, rsrc_t rs, unsigned soff) {
    asm volatile("buffer_store_dwordx2 %0, %1, %2, %3 offen sc1" ::"v"(v), "v"(voff), "s"(rs), "s"(soff) : "memory");
}
template <int N> DI void waitv() { asm volatile("s_waitcnt vmcnt(%0)" ::"n"(N) : "memory"); }
template <int N> DI void waitv_bar() { asm volatile("s_waitcnt vmcnt(%0) lgkmcnt(0)\n\ts_barrier" ::"n"(N) : "memory"); }
template <int N> DI void waitv_tie(u32x4& a) { asm volatile("s_waitcnt vmcnt(%1)" : "+v"(a) : "n"(N) : "memory"); }
template <int N> DI void waitv_tie2(u32x2& a) { asm volatile("s_waitcnt vmcnt(%1)" : "+v"(a) : "n"(N) : "memory"); }
DI void TIE(u32x4& x) { asm volatile("" : "+v"(x)); }
DI void TIE(u32x2& x) { asm volatile("" : "+v"(x)); }
#define BAR_L() asm volatile("s_waitcnt lgkmcnt(0)\n\ts_barrier" ::: "memory")
DI s16x4 trd(const LAS char* p) { return __builtin_bit_cast(s16x4, __builtin_amdgcn_ds_read_tr16_b64_v4i16((LAS s16x4*)p)); }
DI bf16x8 cat8(s16x4 lo, s16x4 hi) { return __builtin_shufflevector(lo, hi, 0, 1, 2, 3, 4, 5, 6, 7); }
DI rsrc_t mkrs(const void* p, unsigned bytes) { return __builtin_amdgcn_make_buffer_rsrc((void*)p, 0, (int)bytes, 0x00020000); }
DI unsigned umax3(unsigned a, unsigned b, unsigned c) { return max(max(a, b), c); }
DI unsigned anysent(const u32x4& a) { const unsigned a0 = a[0], a1 = a[1], a2 = a[2], a3 = a[3]; return max(max(a0, a1), max(a2, a3)); }

#define STAMP(i) do { } while (0)

template <int I> struct IC { static constexpr int v = I; };
template <int B, int E, class F> DI void sfor(F&& f) { if constexpr (B < E) { f(IC<B>{}); sfor<B + 1, E>(f); } }

DI void block_map(int b, int& rb, int& hp) { const int xg = b & 7, idx = b >> 3; rb = (xg >> 2) * 8 + (idx >> 2); hp = (xg & 3) * 4 + (idx & 3); }

constexpr int L_XS0 = 0, L_XS1 = 32768, L_GW = 65536, L_GP = 66560;
constexpr int L_PR = 81920;
constexpr int L_KST = 0, L_VST = 24576, L_QX = 49152, L_ONE = 53248, L_CNT = 53504;
constexpr int L_FR = 57344;
constexpr int LDS_BYTES = 163840;
__host__ __device__ constexpr int fslot(int c) { constexpr int t[13] = {0, 1, 2, 7, 8, 9, 10, 11, 12, 3, 4, 5, 6}; return t[c % 13]; }

__global__ __launch_bounds__(512, 2) void k_fused(const void* x, const void* wqkv, const void* wo, const void* w1, const void* w2, const void* gw, char* ws, void* out) {
    extern __shared__ __attribute__((aligned(16))) char smem[];
    LAS char* L = (LAS char*)smem;
    const int tid = threadIdx.x, lane = tid & 63;
    int w = __builtin_amdgcn_readfirstlane(tid >> 6);
    asm volatile("" : "+s"(w));
    const int m = w >> 1, nh = w & 1, r = lane & 31, h = lane >> 5;
    int rb, hp; block_map(blockIdx.x, rb, hp);
    const unsigned ldsb = (unsigned)(uintptr_t)L;
    STAMP(0);
    unsigned long long epoch;
    asm volatile("s_load_dwordx2 %0, %1, 0x0" : "=s"(epoch) : "s"(ws) : "memory");
    const rsrc_t rx = mkrs(x, S_ * D_ * 2), rq = mkrs(wqkv, D_ * 3 * D_ * 2), r1 = mkrs(w1, D_ * DFF * 2), rg = mkrs(gw, D_ * 2);
    const rsrc_t r2 = mkrs(w2, DFF * D_ * 2), ro = mkrs(wo, D_ * D_ * 2), rw = mkrs(ws, WS_END);
    const unsigned l16 = (unsigned)lane * 16u;
    const int tb = rb * 4 + m;
    const int q = (lane & 15) >> 2, p = lane & 3, blk = (lane >> 4) & 1;

    unsigned voX;
    { const int row = 8 * w + (lane >> 3), pos = lane & 7, c = pos ^ ((row >> 1) & 7); voX = (unsigned)(row * 1024 + c * 16); }
    unsigned voKV;
    { const int row = lane >> 3, pos = lane & 7, c = pos ^ (((row >> 1) & 1) << 2); voKV = (unsigned)((16 * w + row) * 3072 + 1024 + (hp & 1) * 1024 + (hp >> 1) * 128 + c * 16); }
    const int u0 = (hp * 3 < 40) ? hp * 3 : 40;
    unsigned vo2[2];
#pragma unroll
    for (int t = 0; t < 2; ++t) {
        const int e = 2 * w + t;
        if (w < 2) { const int row = 16 * e + (lane >> 2); vo2[t] = (unsigned)(row * 3072 + hp * 64 + (lane & 3) * 16); }
        else { const int li = 64 * (e - 4) + lane, row = li / 12, c = li - row * 12; vo2[t] = (unsigned)(row * 2752 + u0 * 64 + c * 16); }
    }
    const rsrc_t r2p = w < 2 ? rq : r1;
    const unsigned str2 = w < 2 ? 64u * 3072u : 64u * 2752u;
    const unsigned sX = (unsigned)rb * 128u * 1024u;
    auto issueX = [&](int sc) {
        const unsigned dst = ldsb + ((sc & 1) ? L_XS1 : L_XS0) + (unsigned)w * 1024u;
#pragma unroll
        for (int t = 0; t < 4; ++t) dma16(dst + (unsigned)t * 8192u, voX, rx, sX + (unsigned)sc * 256u + (unsigned)(t & 1) * 65536u + (unsigned)(t >> 1) * 128u);
    };
    auto issueKV = [&](int sc) {
        const unsigned dst = ldsb + L_PR + (unsigned)sc * 16384u + (unsigned)w * 2048u;
#pragma unroll
        for (int t = 0; t < 2; ++t) dma16(dst + (unsigned)t * 1024u, voKV, rq, (unsigned)(sc * 128 + 8 * t) * 3072u);
    };
    auto issueP2 = [&](int c) {
        const unsigned dst = ldsb + L_PR + (unsigned)((4 + c) % 5) * 16384u + (unsigned)w * 2048u;
#pragma unroll
        for (int t = 0; t < 2; ++t) dma16(dst + (unsigned)t * 1024u, vo2[t], r2p, (unsigned)c * str2);
    };
    const int rh = hp >> 3, ct = hp & 7;
    unsigned voW, voO;
    { const int row = lane >> 3, pos = lane & 7, c = pos ^ (((row >> 1) & 1) << 2); voW = (unsigned)((8 * w + row) * 1024 + ct * 128 + c * 16);
      const int a = w & 1, hh = row >> 2, qq = row & 3;
      voO = (unsigned)((16 * (w >> 1) + 8 * hh + 4 * a + qq) * 1024 + ct * 128 + c * 16); }
    auto issueF = [&](int c) {
        const unsigned dst = ldsb + L_FR + (unsigned)fslot(c) * 8192u + (unsigned)w * 1024u;
        if (c < 22) dma16(dst, voW, r2, (unsigned)c * 65536u);
        else dma16(dst, voO, ro, (unsigned)(c - 22) * 65536u);
    };

    if (lane < 8) dma16(ldsb + L_GW + (unsigned)w * 128u, (unsigned)(w * 128 + lane * 16), rg, 0u);
    issueX(0); issueKV(0); issueX(1); issueKV(1); issueKV(2); issueKV(3); issueP2(0);
    const unsigned sKV = ((hp & 1) ? WS_V : WS_K) + (unsigned)((((hp & ~1) + nh) * 64 + tb) * 2048);
    const unsigned sO = WS_A + (unsigned)((tb * NKS + NKS_H + hp * 2 + nh) * 1024);
    {
        const u32x4 sv = {SENT, SENT, SENT, SENT};
        st16c(sv, l16, rw, sKV); st16c(sv, l16, rw, sKV + 1024u); st16c(sv, l16, rw, sO);
    }

    const int xrow = m * 32 + r;
    unsigned xoff[4];
#pragma unroll
    for (int s = 0; s < 4; ++s) { const int c = 2 * s + h; xoff[s] = (unsigned)(xrow * 128 + ((c ^ ((xrow >> 1) & 7)) * 16)); }
    const unsigned kvoff = (unsigned)((8 * h + q) * 128 + ((nh * 4 + blk * 2 + (p >> 1)) ^ (((q >> 1) & 1) << 2)) * 16 + (p & 1) * 8);

    bf16x8 xf[32];
    f32x16 accA, accB;
#pragma unroll
    for (int j = 0; j < 16; ++j) { accA[j] = 0.f; accB[j] = 0.f; }
    float gsum = 0.f;

    auto readX = [&](int sc) {
        const LAS char* base = L + ((sc & 1) ? L_XS1 : L_XS0);
#pragma unroll
        for (int c = 0; c < 2; ++c)
#pragma unroll
            for (int s = 0; s < 4; ++s) xf[sc * 8 + c * 4 + s] = *(const LAS bf16x8*)(base + c * 16384 + xoff[s]);
    };
    auto dot8 = [&](const bf16x8& xv, int kpos) {
        const bf16x8 gwf = *(const LAS bf16x8*)(L + L_GW + (kpos + 8 * h) * 2);
        const u32x4 a = __builtin_bit_cast(u32x4, xv), b = __builtin_bit_cast(u32x4, gwf);
#pragma unroll
        for (int j = 0; j < 4; ++j) { const unsigned aj = a[j], bj = b[j]; gsum = __builtin_amdgcn_fdot2_f32_bf16(__builtin_bit_cast(bf16x2_t, aj), __builtin_bit_cast(bf16x2_t, bj), gsum, false); }
    };
    auto gate = [&](int sc) {
#pragma unroll
        for (int c = 0; c < 2; ++c) {
            const bf16x8 a0 = xf[sc * 8 + c * 4 + 0], a1 = xf[sc * 8 + c * 4 + 1], a2 = xf[sc * 8 + c * 4 + 2], a3 = xf[sc * 8 + c * 4 + 3];
            dot8(nh ? a2 : a0, sc * 128 + c * 64 + nh * 32); dot8(nh ? a3 : a1, sc * 128 + c * 64 + nh * 32 + 16);
        }
    };
    auto computeKV = [&](int sc) {
        const LAS char* wb = L + L_PR + sc * 16384 + kvoff;
#pragma unroll
        for (int ks = 0; ks < 8; ++ks) accA = MFMA32(cat8(trd(wb + ks * 2048), trd(wb + ks * 2048 + 512)), xf[sc * 8 + ks], accA);
    };

    waitv_bar<15>(); STAMP(1); readX(0); BAR_L(); issueX(2); gate(0); computeKV(0);
    waitv_bar<13>(); issueP2(1); readX(1); BAR_L(); issueX(3); gate(1); computeKV(1);
    waitv_bar<6>();
    asm volatile("" : "+s"(epoch));
    const unsigned long long tok64 = epoch * 0x9E3779B97F4A7C15ull + 0x632BE59BD9B4E019ull;
    const u32x2 tok = {(unsigned)tok64, (unsigned)(tok64 >> 32)};
    if (tid == 0) st8c(tok, 0u, rw, WS_FC + (unsigned)(rb * 16 + hp) * 8u);
    issueP2(2); readX(2); gate(2); computeKV(2);
    waitv_bar<2>(); issueP2(3); readX(3); gate(3); computeKV(3);
    STAMP(2);
    {
        const float g2 = gsum + __shfl_xor(gsum, 32);
        if (h == 0) ((LAS float*)(L + L_GP))[nh * 128 + xrow] = g2;
        const unsigned ql = (hp & 1) ? (unsigned)(2 * r + h) * 16u : l16;
        st16c(nosent(pack8(accA, 0, 1.f)), ql, rw, sKV);
        st16c(nosent(pack8(accA, 1, 1.f)), ql, rw, sKV + 1024u);
    }
    unsigned voFC;
    { const int l = lane & 31; int frb = rb, fhp = l & 15;
      if (l >= 16 && l < 20) { const int d = 1 + ((l - 16) >> 1); if (rb - d >= 0) { frb = rb - d; fhp = (hp & ~1) + (l & 1); } }
      voFC = (unsigned)(frb * 16 + fhp) * 8u; }
    u32x2 fcv;
    ld8c(fcv, voFC, rw, WS_FC);
#pragma unroll
    for (int j = 0; j < 16; ++j) accA[j] = 0.f;

    const unsigned pitchA = nh ? 192u : 64u;
    const unsigned p2a = (unsigned)(8 * h + q) * pitchA + (nh ? 4096u + 64u : 0u) + (unsigned)(blk * 32 + p * 8);
    const unsigned p2b = (unsigned)(8 * h + q) * 192u + 4096u + (nh ? 128u : 0u) + (unsigned)(blk * 32 + p * 8);
    auto computeP2 = [&](int c) {
        const LAS char* wb = L + L_PR + ((4 + c) % 5) * 16384;
#pragma unroll
        for (int s = 0; s < 4; ++s) {
            const bf16x8 xx = xf[c * 4 + s];
            accA = MFMA32(cat8(trd(wb + p2a + (unsigned)(s * 16) * pitchA), trd(wb + p2a + (unsigned)(s * 16 + 4) * pitchA)), xx, accA);
            accB = MFMA32(cat8(trd(wb + p2b + s * 16 * 192), trd(wb + p2b + (s * 16 + 4) * 192)), xx, accB);
        }
    };
    BAR_L(); issueP2(4);
    const float gz = ((const LAS float*)(L + L_GP))[xrow] + ((const LAS float*)(L + L_GP))[128 + xrow];
    const float g = 1.f / (1.f + __expf(-gz));
    if (tid < 64) ((LAS unsigned*)(L + L_ONE))[tid] = 0x3f803f80u;
    if (tid == 64) *(LAS unsigned*)(L + L_CNT) = 0u;
    computeP2(0);
    BAR_L(); issueP2(5); issueF(0); issueF(1); issueF(2); computeP2(1);
    waitv_bar<12>(); issueP2(6); computeP2(2);
    waitv_bar<12>(); issueP2(7); computeP2(3);
    waitv_bar<9>();
    {
        TIE(fcv);
        unsigned spins = 0;
        while (!__all(fcv[0] == tok[0] && fcv[1] == tok[1]) && ++spins < SPIN_CAP) { __builtin_amdgcn_s_sleep(2); ld8c(fcv, voFC, rw, WS_FC); waitv_tie2<0>(fcv); }
    }
    issueF(3); issueF(4); computeP2(4);
    waitv_bar<9>(); issueF(5); issueF(6); computeP2(5);
    waitv_bar<6>(); issueF(7); issueF(8);
    u32x4 kst[3], vst[3];
    unsigned sst[3];
#pragma unroll
    for (int t = 0; t < 3; ++t) {
        const int pc = w + 8 * t, i = pc >> 1, hh = pc & 1, kt = rb * 4 - 8 + i;
        sst[t] = (unsigned)(((hp * 64 + (kt < 0 ? tb : kt)) * 2 + (kt < 0 ? 0 : hh)) * 1024);
        ld16c(kst[t], l16, rw, WS_K + sst[t]); ld16c(vst[t], l16, rw, WS_V + sst[t]);
    }
    computeP2(6);
    waitv_bar<12>(); issueF(9); issueF(10); computeP2(7);
    STAMP(3);
    bf16x8 qf;
    {
        const float og = 1.f - g;
        const unsigned sA = WS_A + (unsigned)(tb * NKS + 2 * u0) * 1024u;
#pragma unroll
        for (int j = 0; j < 16; ++j) accB[j] = fmaxf(accB[j], 0.f);
        st16c(pack8(accB, 0, og), l16, rw, sA + (nh ? 4096u : 0u));
        st16c(pack8(accB, 1, og), l16, rw, sA + (nh ? 5120u : 1024u));
        if (nh) {
#pragma unroll
            for (int j = 0; j < 16; ++j) accA[j] = fmaxf(accA[j], 0.f);
            st16c(pack8(accA, 0, og), l16, rw, sA + 2048u);
            st16c(pack8(accA, 1, og), l16, rw, sA + 3072u);
        } else {
            qf = __builtin_bit_cast(bf16x8, pack8(accA, 0, QSCALE));
            *(LAS u32x4*)(L + L_QX + m * 1024 + l16) = pack8(accA, 1, QSCALE);
        }
    }
    {
        if (nh) waitv<6>(); else waitv<4>();
#pragma unroll
        for (int t = 0; t < 3; ++t) { TIE(kst[t]); TIE(vst[t]); }
        unsigned spins = 0;
        for (;;) {
            unsigned mxs = 0;
#pragma unroll
            for (int t = 0; t < 3; ++t) mxs = max(mxs, max(anysent(kst[t]), anysent(vst[t])));
            if (__all(mxs != SENT) || ++spins >= SPIN_CAP) break;
            __builtin_amdgcn_s_sleep(1);
#pragma unroll
            for (int t = 0; t < 3; ++t) { ld16c(kst[t], l16, rw, WS_K + sst[t]); ld16c(vst[t], l16, rw, WS_V + sst[t]); }
            waitv<0>();
#pragma unroll
            for (int t = 0; t < 3; ++t) { TIE(kst[t]); TIE(vst[t]); }
        }
#pragma unroll
        for (int t = 0; t < 3; ++t) {
            const int pc = w + 8 * t, i = pc >> 1, hh = pc & 1;
            *(LAS u32x4*)(L + L_KST + (hh * 12 + i) * 1024 + l16) = kst[t];
            *(LAS u32x4*)(L + L_VST + (hh * 12 + i) * 1024 + l16) = vst[t];
        }
    }
    BAR_L(); issueF(11); issueF(12);
    STAMP(4);

    {
        if (nh) qf = *(const LAS bf16x8*)(L + L_QX + m * 1024 + l16);
        const int t0 = tb >= 8 ? 0 : 8 - tb;
        const LAS char* kb = L + L_KST + (nh * 12 + m) * 1024 + l16;
        const LAS char* vb = blk ? (L + L_ONE + (1 - h) * 128) : (L + L_VST + (nh * 12 + m) * 1024 + (4 * h + q) * 32 + p * 8);
        const int vstep = blk ? 0 : 1;
        bf16x8 kf[9];
#pragma unroll
        for (int i = 0; i < 9; ++i) kf[i] = *(const LAS bf16x8*)(kb + i * 1024);
        f32x16 zero;
#pragma unroll
        for (int j = 0; j < 16; ++j) zero[j] = 0.f;
        const float NEG = -INFINITY;
        float mx = NEG;
#pragma unroll
        for (int i = 0; i < 9; ++i) {
            if (i >= t0) {
                f32x16 s = MFMA32(kf[i], qf, zero);
#pragma unroll
                for (int j = 0; j < 16; ++j) {
                    const int key = (j & 3) + 8 * (j >> 2) + 4 * h;
                    float v = s[j];
                    if (i == 0) v = key > r ? v : NEG;
                    if (i == 8) v = key <= r ? v : NEG;
                    mx = fmaxf(mx, v);
                }
            }
        }
        mx = fmaxf(mx, __shfl_xor(mx, 32));
        f32x16 negm;
#pragma unroll
        for (int j = 0; j < 16; ++j) negm[j] = -mx;
        f32x16 o = zero;
        u32x2 fhv;
#pragma unroll
        for (int i = 0; i < 9; ++i) {
            if (i >= t0) {
                f32x16 s = MFMA32(kf[i], qf, negm);
#pragma unroll
                for (int j = 0; j < 16; ++j) {
                    const int key = (j & 3) + 8 * (j >> 2) + 4 * h;
                    float pe = __builtin_amdgcn_exp2f(s[j]);
                    if (i == 0) pe = key > r ? pe : 0.f;
                    if (i == 8) pe = key <= r ? pe : 0.f;
                    s[j] = pe;
                }
#pragma unroll
                for (int ss = 0; ss < 2; ++ss) {
                    const LAS char* p0 = vb + vstep * (i * 1024 + ss * 512);
                    const bf16x8 vf = cat8(trd(p0), trd(p0 + vstep * 256));
                    o = MFMA32(vf, __builtin_bit_cast(bf16x8, pack8(s, ss, 1.f)), o);
                }
            }
            if (i == 4) {
                waitv<2>();
                unsigned cnt = 0;
                if (lane == 0) cnt = __hip_atomic_fetch_add((LAS unsigned*)(L + L_CNT), 1u, __ATOMIC_RELAXED, __HIP_MEMORY_SCOPE_WORKGROUP);
                cnt = __builtin_amdgcn_readfirstlane(cnt);
                if (cnt == 7u && lane == 0) st8c(tok, 0u, rw, WS_FH + (unsigned)(rb * 16 + hp) * 8u);
            }
            if (i == 7) ld8c(fhv, (unsigned)(rb * 16 + (lane & 15)) * 8u, rw, WS_FH);
        }
        const float sc = g / o[8];
        st16c(nosent(pack8(o, 0, sc)), l16, rw, sO);
        STAMP(5);
        waitv_tie2<1>(fhv);
        unsigned spins = 0;
        while (!__all(fhv[0] == tok[0] && fhv[1] == tok[1]) && ++spins < SPIN_CAP) { __builtin_amdgcn_s_sleep(1); ld8c(fhv, (unsigned)(rb * 16 + (lane & 15)) * 8u, rw, WS_FH); waitv_tie2<0>(fhv); }
    }
    STAMP(6);

    {
        const int fm = w & 1, kq = w >> 1;
        const unsigned sAf = WS_A + (unsigned)((rb * 4 + rh * 2 + fm) * NKS + kq) * 1024u;
        u32x4 af[30];
        u32x2 ev[2];
        const bool boss = blockIdx.x == 0 && w == 0;
        u32x4 ea[2];
        (void)ev;
        if (boss) { ld16c(ea[0], l16, rw, WS_FC); ld16c(ea[1], l16, rw, WS_FC + 1024u); }
        sfor<0, 22>([&](auto C) { constexpr int c = decltype(C)::v; ld16c(af[c], l16, rw, sAf + (unsigned)((c == 21 && kq >= 2) ? 0 : 4 * c) * 1024u); });
        unsigned woff[2];
#pragma unroll
        for (int t = 0; t < 2; ++t) { const int row = 16 * kq + 4 * h + q; const int c16 = (t * 4 + blk * 2 + (p >> 1)) ^ (((row >> 1) & 1) << 2); woff[t] = (unsigned)(row * 128 + c16 * 16 + (p & 1) * 8); }
        f32x16 acc[2];
#pragma unroll
        for (int i = 0; i < 2; ++i)
#pragma unroll
            for (int j = 0; j < 16; ++j) acc[i][j] = 0.f;
        auto mm = [&](auto C) {
            constexpr int c = decltype(C)::v;
            if (c != 21 || kq < 2) {
                const LAS char* wb = L + L_FR + fslot(c) * 8192;
#pragma unroll
                for (int t = 0; t < 2; ++t) {
                    const bf16x8 wf = cat8(trd(wb + woff[t]), trd(wb + woff[t] + 8 * 128));
                    acc[t] = MFMA32(wf, __builtin_bit_cast(bf16x8, af[c]), acc[t]);
                }
            }
        };
        bool epoch_done = !boss;
        sfor<0, 4>([&](auto C) { constexpr int c = decltype(C)::v; waitv_tie<21 - c>(af[c]); mm(C); });
        if (boss) {
            TIE(ea[0]); TIE(ea[1]);
            bool ok = true;
#pragma unroll
            for (int j = 0; j < 2; ++j) { const unsigned a0 = ea[j][0], a1 = ea[j][1], a2 = ea[j][2], a3 = ea[j][3]; ok = ok && a0 == tok[0] && a1 == tok[1] && a2 == tok[0] && a3 == tok[1]; }
            if (__all(ok)) { epoch_done = true; if (lane == 0) { const unsigned long long e1 = epoch + 1; const u32x2 ev1 = {(unsigned)e1, (unsigned)(e1 >> 32)}; st8c(ev1, 0u, rw, WS_E); } }
        }
        BAR_L(); issueF(13); issueF(14); issueF(15); issueF(16);
        sfor<4, 9>([&](auto C) { constexpr int c = decltype(C)::v; waitv_tie<21 - c + 4>(af[c]); mm(C); });
        BAR_L(); issueF(17); issueF(18); issueF(19); issueF(20); issueF(21);
        sfor<9, 13>([&](auto C) { constexpr int c = decltype(C)::v; waitv_tie<21 - c + 9>(af[c]); mm(C); });
        BAR_L(); issueF(22); issueF(23); issueF(24); issueF(25);
        STAMP(7);
        waitv_tie<21 - 16 + 13>(af[16]); TIE(af[13]); TIE(af[14]); TIE(af[15]);
        waitv_bar<5 + 4>();
        sfor<13, 17>([&](auto C) { mm(C); });
        sfor<22, 30>([&](auto C) { constexpr int c = decltype(C)::v; ld16c(af[c], l16, rw, sAf + (unsigned)(NKS_H + 4 * (c - 22)) * 1024u); });
        BAR_L(); issueF(26); issueF(27); issueF(28); issueF(29);
        waitv_bar<16>();
        TIE(af[17]); TIE(af[18]); TIE(af[19]); TIE(af[20]); TIE(af[21]);
        sfor<17, 22>([&](auto C) { mm(C); });
        STAMP(8);
        waitv<4>();
        sfor<22, 30>([&](auto C) { TIE(af[decltype(C)::v]); });
        {
            unsigned spins = 0;
            for (;;) {
                unsigned mxs = 0;
                sfor<22, 30>([&](auto C) { mxs = max(mxs, anysent(af[decltype(C)::v])); });
                if (__all(mxs != SENT) || ++spins >= SPIN_CAP) break;
                __builtin_amdgcn_s_sleep(1);
                sfor<22, 30>([&](auto C) { constexpr int c = decltype(C)::v; ld16c(af[c], l16, rw, sAf + (unsigned)(NKS_H + 4 * (c - 22)) * 1024u); });
                waitv<0>();
                sfor<22, 30>([&](auto C) { TIE(af[decltype(C)::v]); });
            }
        }
        STAMP(9);
        waitv_bar<0>();
        sfor<22, 30>([&](auto C) { mm(C); });
        BAR_L();
        STAMP(10);
        constexpr int RP = 272;
        {
            LAS char* dst = L + kq * (64 * RP) + (fm * 32 + r) * RP;
#pragma unroll
            for (int t = 0; t < 2; ++t)
#pragma unroll
                for (int g4 = 0; g4 < 4; ++g4)
                    *(LAS f32x4*)(dst + (t * 32 + g4 * 8 + 4 * h) * 4) = (f32x4){acc[t][4 * g4], acc[t][4 * g4 + 1], acc[t][4 * g4 + 2], acc[t][4 * g4 + 3]};
        }
        BAR_L();
        {
            const int tok_ = tid >> 3, oc = tid & 7;
            f32x4 a = {0.f, 0.f, 0.f, 0.f}, b = a;
#pragma unroll
            for (int k = 0; k < 4; ++k) {
                const LAS char* src = L + k * (64 * RP) + tok_ * RP + oc * 32;
                a += *(const LAS f32x4*)src; b += *(const LAS f32x4*)(src + 16);
            }
            u32x4 o = {cvtpk(a[0], a[1]), cvtpk(a[2], a[3]), cvtpk(b[0], b[1]), cvtpk(b[2], b[3])};
            *(GAS u32x4*)((char*)out + ((size_t)(rb * 128 + rh * 64 + tok_) * 512 + ct * 64 + oc * 8) * 2) = o;
        }
        if (!epoch_done) {
            unsigned spins = 0;
            for (;;) {
                ld16c(ea[0], l16, rw, WS_FC); ld16c(ea[1], l16, rw, WS_FC + 1024u); waitv<0>(); TIE(ea[0]); TIE(ea[1]);
                bool ok = true;
#pragma unroll
                for (int j = 0; j < 2; ++j) { const unsigned a0 = ea[j][0], a1 = ea[j][1], a2 = ea[j][2], a3 = ea[j][3]; ok = ok && a0 == tok[0] && a1 == tok[1] && a2 == tok[0] && a3 == tok[1]; }
                if (__all(ok) || ++spins >= SPIN_CAP) break;
                __builtin_amdgcn_s_sleep(2);
            }
            if (lane == 0) { const unsigned long long e1 = epoch + 1; const u32x2 ev1 = {(unsigned)e1, (unsigned)(e1 >> 32)}; st8c(ev1, 0u, rw, WS_E); }
        }
    }
}

extern "C" void kernel_launch(void* const* d_in, const int* in_sizes, int n_in, void* d_out, int out_size, void* d_ws, size_t ws_size, hipStream_t stream) {
    (void)hipFuncSetAttribute((const void*)k_fused, hipFuncAttributeMaxDynamicSharedMemorySize, LDS_BYTES);
    k_fused<<<256, 512, LDS_BYTES, stream>>>(d_in[0], d_in[1], d_in[2], d_in[4], d_in[5], d_in[6], (char*)d_ws, d_out);
}
```

```cpp
#include <hip/hip_runtime.h>
#include <hip/hip_bf16.h>
#include <stdint.h>

#define DI __device__ __forceinline__
#define LAS __attribute__((address_space(3)))
#define GAS __attribute__((address_space(1)))
typedef short bf16x8 __attribute__((ext_vector_type(8)));
typedef short s16x4 __attribute__((ext_vector_type(4)));
typedef float f32x16 __attribute__((ext_vector_type(16)));
typedef float f32x4 __attribute__((ext_vector_type(4)));
typedef float f32x2 __attribute__((ext_vector_type(2)));
typedef __bf16 bf16x2_t __attribute__((ext_vector_type(2)));
typedef unsigned u32x4 __attribute__((ext_vector_type(4)));
typedef unsigned u32x2 __attribute__((ext_vector_type(2)));
typedef __amdgpu_buffer_rsrc_t rsrc_t;

constexpr int S_ = 2048, D_ = 512, DFF = 1376;
constexpr int NKS_H = DFF / 16;
constexpr int NKS = NKS_H + 32;
constexpr unsigned WS_E = 0;
constexpr unsigned WS_FH = 8192;
constexpr unsigned WS_FK = 16384;
constexpr unsigned WS_K = 1u << 20;
constexpr unsigned WS_V = WS_K + (2u << 20);
constexpr unsigned WS_A = WS_V + (2u << 20);
constexpr unsigned WS_END = WS_A + 64u * NKS * 1024u;
constexpr unsigned WS_DBG = WS_END;
constexpr unsigned SENT = 0xFFFFFFFFu;
constexpr unsigned SPIN_CAP = 1u << 18;

constexpr float QSCALE = 0.25f * 1.4426950408889634f;

#define MFMA32(a, b, c) __builtin_amdgcn_mfma_f32_32x32x16_bf16((a), (b), (c), 0, 0, 0)

DI unsigned cvtpk(float lo, float hi) { f32x2 v = {lo, hi}; bf16x2_t b = __builtin_convertvector(v, bf16x2_t); return __builtin_bit_cast(unsigned, b); }
DI u32x4 pack8(const f32x16& x, int s, float sc) {
    u32x4 p;
    p[0] = cvtpk(x[8 * s + 0] * sc, x[8 * s + 1] * sc); p[1] = cvtpk(x[8 * s + 2] * sc, x[8 * s + 3] * sc);
    p[2] = cvtpk(x[8 * s + 4] * sc, x[8 * s + 5] * sc); p[3] = cvtpk(x[8 * s + 6] * sc, x[8 * s + 7] * sc);
    return p;
}
DI u32x4 nosent(u32x4 p) {
#pragma unroll
    for (int j = 0; j < 4; ++j) { const unsigned v = p[j]; p[j] = v == SENT ? 0x7FC07FC0u : v; }
    return p;
}
DI void dma16(unsigned lds_dst, unsigned voff, rsrc_t rs, unsigned soff) {
    asm volatile("s_mov_b32 m0, %0\n\ts_nop 0\n\tbuffer_load_dwordx4 %1, %2, %3 offen lds" ::"s"(lds_dst), "v"(voff), "s"(rs), "s"(soff) : "memory");
}
DI void ld16c(u32x4& v, unsigned voff, rsrc_t rs, unsigned soff) {
    asm volatile("buffer_load_dwordx4 %0, %1, %2, %3 offen sc1" : "=&v"(v) : "v"(voff), "s"(rs), "s"(soff) : "memory");
}
DI void ld8c(u32x2& v, unsigned voff, rsrc_t rs, unsigned soff) {
    asm volatile("buffer_load_dwordx2 %0, %1, %2, %3 offen sc1" : "=&v"(v) : "v"(voff), "s"(rs), "s"(soff) : "memory");
}
DI void st16c(u32x4 v, unsigned voff, rsrc_t rs, unsigned soff) {
    asm volatile("buffer_store_dwordx4 %0, %1, %2, %3 offen sc1\n\ts_nop 1" ::"v"(v), "v"(voff), "s"(rs), "s"(soff) : "memory");
}
DI void st8c(u32x2 v, unsigned voff, rsrc_t rs, unsigned soff) {
    asm volatile("buffer_store_dwordx2 %0, %1, %2, %3 offen sc1" ::"v"(v), "v"(voff), "s"(rs), "s"(soff) : "memory");
}
template <int N> DI void waitv() { asm volatile("s_waitcnt vmcnt(%0)" ::"n"(N) : "memory"); }
template <int N> DI void waitv_bar() { asm volatile("s_waitcnt vmcnt(%0) lgkmcnt(0)\n\ts_barrier" ::"n"(N) : "memory"); }
DI void ld_epoch(unsigned long long& e, const char* ws) { asm volatile("s_load_dwordx2 %0, %1, 0x0" : "=s"(e) : "s"(ws) : "memory"); }
template <int N> DI void waitv_bar_e(unsigned long long& e) { asm volatile("s_waitcnt vmcnt(%1) lgkmcnt(0)\n\ts_barrier" : "+s"(e) : "n"(N) : "memory"); }
template <int N> DI void waitv_tie(u32x4& a) { asm volatile("s_waitcnt vmcnt(%1)" : "+v"(a) : "n"(N) : "memory"); }
template <int N> DI void waitv_tie2(u32x2& a) { asm volatile("s_waitcnt vmcnt(%1)" : "+v"(a) : "n"(N) : "memory"); }
DI void TIE(u32x4& x) { asm volatile("" : "+v"(x)); }
DI void TIE(u32x2& x) { asm volatile("" : "+v"(x)); }
#define BAR_L() asm volatile("s_waitcnt lgkmcnt(0)\n\ts_barrier" ::: "memory")
DI s16x4 trd(const LAS char* p) { return __builtin_bit_cast(s16x4, __builtin_amdgcn_ds_read_tr16_b64_v4i16((LAS s16x4*)p)); }
DI bf16x8 cat8(s16x4 lo, s16x4 hi) { return __builtin_shufflevector(lo, hi, 0, 1, 2, 3, 4, 5, 6, 7); }
DI rsrc_t mkrs(const void* p, unsigned bytes) { return __builtin_amdgcn_make_buffer_rsrc((void*)p, 0, (int)bytes, 0x00020000); }
DI unsigned umax3(unsigned a, unsigned b, unsigned c) { return max(max(a, b), c); }
DI unsigned anysent(const u32x4& a) { const unsigned a0 = a[0], a1 = a[1], a2 = a[2], a3 = a[3]; return max(max(a0, a1), max(a2, a3)); }

#define STAMP(i) do { } while (0)
#define STAMPC(i) do { } while (0)
#define STAMPW 0
#define FST(i) do { } while (0)

template <int I> struct IC { static constexpr int v = I; };
template <int B, int E, class F> DI void sfor(F&& f) { if constexpr (B < E) { f(IC<B>{}); sfor<B + 1, E>(f); } }

DI void block_map(int b, int& rb, int& hp) { const int xg = b & 7, idx = b >> 3; rb = (xg >> 2) * 8 + (idx >> 2); hp = (xg & 3) * 4 + (idx & 3); }

constexpr int KB = 1024;
__host__ __device__ constexpr int stS(int g) { return (g == 3 ? 0 : g) * 48 * KB; }
constexpr int L_W1 = 48 * KB;
constexpr int L_WQ = 0;
__host__ __device__ constexpr int fsl(int c) {
    constexpr int t[19] = {32, 40, 144, 48, 56, 64, 72, 128, 136, 0, 8, 16, 24, 80, 88, 96, 104, 112, 120};
    return t[c % 19] * KB;
}
constexpr int L_KST = 80 * KB, L_VST = 104 * KB;
constexpr int L_MISC = 152 * KB;
constexpr int L_GW = L_MISC, L_GP = L_MISC + 1024, L_QX = L_MISC + 2048, L_ONE = L_MISC + 6144, L_CNT = L_MISC + 6400, L_ZERO = L_MISC + 6656;
constexpr int LDS_BYTES = 163840;
#define KVFLAG 16
#define FHS 2

#define BAR_N(n) asm volatile("s_waitcnt lgkmcnt(" #n ")\n\ts_barrier" ::: "memory")
#define SB() __builtin_amdgcn_sched_barrier(0)
typedef short s16x2 __attribute__((ext_vector_type(2)));
DI unsigned relu2(unsigned v) { const s16x2 z = {0, 0}; return __builtin_bit_cast(unsigned, __builtin_elementwise_max(__builtin_bit_cast(s16x2, v), z)); }
DI const LAS char* lp(unsigned a) { return (const LAS char*)(uintptr_t)a; }
DI unsigned opq(unsigned v) { asm volatile("" : "+v"(v)); return v; }

__global__ __launch_bounds__(512, 2) void k_fused(const void* x, const void* wqkv, const void* wo, const void* w1, const void* w2, const void* gw, char* ws, void* out) {
    extern __shared__ __attribute__((aligned(16))) char smem[];
    LAS char* L = (LAS char*)smem;
    const int tid = threadIdx.x, lane = tid & 63;
    int w = __builtin_amdgcn_readfirstlane(tid >> 6);
    asm volatile("" : "+s"(w));
    const bool isC = w < 4;
    const int ww = w & 3, r = lane & 31, h = lane >> 5;
    int rb, hp; block_map(blockIdx.x, rb, hp);
    const int xg = blockIdx.x & 7, idx = blockIdx.x >> 3;
    const int rbF = 2 * xg + (idx >> 4), rh = (idx >> 3) & 1, ct = idx & 7;
    const unsigned ldsb = (unsigned)(uintptr_t)L;
    STAMP(0); STAMPC(12);
    unsigned long long epoch;
    const rsrc_t rx = mkrs(x, S_ * D_ * 2), rq = mkrs(wqkv, D_ * 3 * D_ * 2), r1 = mkrs(w1, D_ * DFF * 2), rg = mkrs(gw, D_ * 2);
    const rsrc_t r2 = mkrs(w2, DFF * D_ * 2), ro = mkrs(wo, D_ * D_ * 2), rw = mkrs(ws, WS_END);
    const unsigned l16 = (unsigned)lane * 16u;
    const int tb = rb * 4 + ww;
    const int q = (lane & 15) >> 2, p = lane & 3, blk = (lane >> 4) & 1;
    const int u0 = (hp * 3 < 40) ? hp * 3 : 40;
    const int row8 = lane >> 3, pos = lane & 7;
    const u32x4 sentv = {SENT, SENT, SENT, SENT};

    const unsigned voX = (unsigned)((8 * ww + row8) * 1024 + (pos ^ (((8 * ww + row8) >> 1) & 7)) * 16);
    const unsigned sX = (unsigned)rb * 128u * 1024u;
    auto issueX = [&](int g) {
#pragma unroll
        for (int t = 0; t < 8; ++t)
            dma16(ldsb + (unsigned)(stS(g) + (t >> 2) * 16384 + (t & 3) * 4096) + (unsigned)ww * 1024u, voX, rx, sX + (unsigned)(g * 256 + (t & 3) * 32768 + (t >> 2) * 128));
    };
    const unsigned voKV = (unsigned)((16 * ww + row8) * 3072 + 1024 + (hp & 1) * 1024 + (hp >> 1) * 128 + (pos ^ (((row8 >> 1) & 1) << 2)) * 16);
    auto issueKV = [&](int j) {
#pragma unroll
        for (int t = 0; t < 2; ++t) dma16(ldsb + (unsigned)(stS(j >> 1) + 32 * KB + (j & 1) * 8192 + t * 1024) + (unsigned)ww * 2048u, voKV, rq, (unsigned)(j * 64 + 8 * t) * 3072u);
    };
    auto mkvoW = [&](int ln) { const int r8 = ln >> 3, ps = ln & 7; return (unsigned)(r8 * 1024 + ct * 128 + (ps ^ (((r8 >> 1) & 1) << 2)) * 16); };
    auto mkvoO = [&](int ln) { const int r8 = ln >> 3, ps = ln & 7; return (unsigned)((8 * (r8 >> 2) + (r8 & 3)) * 1024 + ct * 128 + (ps ^ (((r8 >> 1) & 1) << 2)) * 16); };
    auto issueFp = [&](int c, int pi, unsigned vW, unsigned vO) {
        const unsigned dst = ldsb + (unsigned)fsl(c) + (unsigned)pi * 1024u;
        if (c < 22) dma16(dst, vW, r2, (unsigned)(c * 65536) + (unsigned)pi * 8192u);
        else dma16(dst, vO, ro, (unsigned)((c - 22) * 65536) + (unsigned)(16 * (pi >> 1) + 4 * (pi & 1)) * 1024u);
    };

    unsigned long long tok64 = 0;
    u32x2 tok;
    bf16x8 qf;
    auto mktok = [&]() {
        tok64 = epoch * 0x9E3779B97F4A7C15ull + 0x632BE59BD9B4E019ull;
        tok = (u32x2){(unsigned)tok64, (unsigned)(tok64 >> 32)};
    };

    if (isC) {
        const unsigned sKV = ((hp & 1) ? WS_V : WS_K) + (unsigned)(((hp & ~1) * 64 + tb) * 2048);
        if (w == 0) dma16(ldsb + L_GW, l16, rg, 0u);
        issueKV(0); issueKV(1);
        issueX(0);
        FST(15);
        unsigned long long ep; ld_epoch(ep, ws);
        const int xrow = ww * 32 + r;
        unsigned xoff[4];
#pragma unroll
        for (int s = 0; s < 4; ++s) { const int c = 2 * s + h; xoff[s] = ldsb + (unsigned)(xrow * 128 + ((c ^ ((xrow >> 1) & 7)) * 16)); }
        const unsigned kvoff = (unsigned)((8 * h + q) * 128 + ((blk * 2 + (p >> 1)) ^ (((q >> 1) & 1) << 2)) * 16 + (p & 1) * 8);
        const unsigned gwoff = ldsb + (((lane & 15) == blk) ? (unsigned)(L_GW + h * 16) : (unsigned)L_ZERO);
        bf16x8 xf[32];
        f32x16 acc[2];
#pragma unroll
        for (int j = 0; j < 16; ++j) { acc[0][j] = 0.f; acc[1][j] = 0.f; }
        f32x4 ag = {0.f, 0.f, 0.f, 0.f};
        FST(13);
        waitv_bar_e<0>(ep);
        STAMP(1);
        epoch = ep; mktok();
        sfor<0, 4>([&](auto G) {
            constexpr int g = decltype(G)::v;
            const unsigned hi = stS(g) >= 65536 ? 65536u : 0u;
#pragma unroll
            for (int c = 0; c < 2; ++c)
#pragma unroll
                for (int s = 0; s < 4; ++s) xf[g * 8 + c * 4 + s] = *(const LAS bf16x8*)(lp((stS(g) >= 65536 ? opq(xoff[s] + hi) : xoff[s])) + (stS(g) - (int)hi + c * 16384));
            const unsigned kb0 = opq(ldsb + kvoff + (unsigned)(stS(g) + 32 * KB)), kb1 = opq(ldsb + (kvoff ^ 64u) + (unsigned)(stS(g) + 32 * KB));
            constexpr int DK = 4;
            bf16x8 wr[DK + 1], gf[4];
            sfor<0, 16 + DK>([&](auto I) {
                constexpr int i = decltype(I)::v;
                if constexpr (i < 16) {
                    const LAS char* pw = lp((i & 1) ? kb1 : kb0) + (i >> 3) * 8192 + ((i >> 1) & 3) * 2048;
                    wr[i % (DK + 1)] = cat8(trd(pw), trd(pw + 512));
                    if constexpr ((i & 1) == 0) gf[(i >> 1) & 3] = *(const LAS bf16x8*)(lp(gwoff) + (g * 128 + (i >> 1) * 16) * 2);
                }
                if constexpr (i >= DK) {
                    constexpr int j = i - DK;
                    acc[j & 1] = MFMA32(wr[j % (DK + 1)], xf[g * 8 + (j >> 1)], acc[j & 1]);
                    if constexpr ((j & 1) == 1) ag = __builtin_amdgcn_mfma_f32_16x16x32_bf16(gf[(j >> 1) & 3], xf[g * 8 + (j >> 1)], ag, 0, 0, 0);
                }
                SB();
            });
            FST(g);
            if constexpr (g < 3) BAR_L();
        });
        STAMP(2);
        if (lane < 16) {
            ((LAS float*)(L + L_GP))[ww * 32 + lane] = 1.f / (1.f + __expf(-ag[0]));
            ((LAS float*)(L + L_GP))[ww * 32 + 16 + lane] = 1.f / (1.f + __expf(-ag[1]));
        }
        const float og = 1.f - ((const LAS float*)(L + L_GP))[xrow];
        const unsigned ql = (hp & 1) ? (unsigned)(2 * r + h) * 16u : l16;
        const unsigned w1off = ldsb + (unsigned)((8 * h + q) * 192 + blk * 32 + p * 8);
        const unsigned w1a = opq(w1off + (unsigned)L_W1), w1b = opq(w1off + (unsigned)(L_W1 + 60 * KB));
        const unsigned wqo = opq(ldsb + (unsigned)(L_WQ + (8 * h + q) * 64 + blk * 32 + p * 8));
        const unsigned sA = WS_A + (unsigned)(tb * NKS + 2 * u0) * 1024u;
        const int dup = hp * 3 - u0;
        BAR_L();
        FST(4);
        {
            constexpr int DP = 6, RP_ = 8;
            bf16x8 wr[RP_];
            f32x16 ah[2];
            u32x4 pk;
            sfor<0, 128 + DP>([&](auto I) {
                constexpr int i = decltype(I)::v;
                if constexpr (i == 16) { FST(5); BAR_N(12); FST(6); }
                if constexpr (i == 84) { FST(22); BAR_N(12); FST(23); }
                if constexpr (i == 96) { FST(7); BAR_N(4); FST(8); }
                if constexpr (i < 96) {
                    constexpr int t = i / 32, c = (i % 32) / 4, s = i % 4;
                    const LAS char* pw = lp(c < 5 ? w1a : w1b) + ((c < 5 ? c : c - 5) * 12 * KB + s * 16 * 192 + t * 64);
                    wr[i % RP_] = cat8(trd(pw), trd(pw + 4 * 192));
                } else if constexpr (i < 128) {
                    const LAS char* pw = lp(wqo) + (i - 96) * 1024;
                    wr[i % RP_] = cat8(trd(pw), trd(pw + 256));
                }
                if constexpr (i >= DP) {
                    constexpr int j = i - DP, T = j / 32, jj = j % 32;
                    if constexpr (jj == 0) {
                        f32x16 z;
#pragma unroll
                        for (int e = 0; e < 16; ++e) z[e] = 0.f;
                        ah[T & 1] = MFMA32(wr[j % RP_], xf[jj], z);
                    } else ah[T & 1] = MFMA32(wr[j % RP_], xf[jj], ah[T & 1]);
                    if constexpr (T == 0 && jj < 16) {
                        constexpr int n = jj / 4, qd = jj % 4, t = n >> 1, s = n & 1;
                        pk[qd] = cvtpk(acc[t][8 * s + 2 * qd], acc[t][8 * s + 2 * qd + 1]);
                        if constexpr (qd == 3) st16c(pk, ql, rw, sKV + (unsigned)(t * 131072 + s * 1024));
                    }
                    if constexpr (T == 1 && jj == KVFLAG) {
                        FST(20);
                        waitv<(KVFLAG >= 9) ? 2 : (KVFLAG >= 5) ? 1 : 0>();
                        FST(21);
                        if (lane == 0) st8c(tok, 0u, rw, WS_FK + (unsigned)(tb * 16 + hp) * 8u);
                    }
                    if constexpr (T >= 1 && jj >= 1 && jj < 9) {
                        constexpr int n = (jj - 1) / 4, qd = (jj - 1) % 4, t = T - 1;
                        pk[qd] = relu2(cvtpk(ah[t & 1][8 * n + 2 * qd] * og, ah[t & 1][8 * n + 2 * qd + 1] * og));
                        if constexpr (qd == 3) { if (t == 0 || t >= dup) st16c(pk, l16, rw, sA + (unsigned)((2 * t + n) * 1024)); }
                    }
                }
                SB();
            });
            qf = __builtin_bit_cast(bf16x8, pack8(ah[1], 0, QSCALE));
            *(LAS u32x4*)(L + L_QX + ww * 1024 + l16) = pack8(ah[1], 1, QSCALE);
        }
        STAMP(3);
        BAR_L();
    } else {
        issueX(1); issueKV(2); issueKV(3);
        FST(15);
        unsigned long long ep; ld_epoch(ep, ws);
        if (w == 5 && lane == 0) *(LAS unsigned*)(L + L_CNT) = 0u;
        if (w == 6) *(LAS u32x4*)(L + L_ZERO + l16) = (u32x4){0u, 0u, 0u, 0u};
        unsigned voW1[3];
#pragma unroll
        for (int t = 0; t < 3; ++t) { const int li = 64 * t + lane, row = li / 12, c = li - row * 12; voW1[t] = (unsigned)((16 * ww + row) * 2752 + u0 * 64 + c * 16); }
        const unsigned voQ = (unsigned)((32 * ww + (lane >> 2)) * 3072 + hp * 64 + (lane & 3) * 16);
        auto issueW1 = [&](int c) {
#pragma unroll
            for (int t = 0; t < 3; ++t) dma16(ldsb + (unsigned)(L_W1 + c * 12 * KB + t * 1024) + (unsigned)ww * 3072u, voW1[t], r1, (unsigned)c * 64u * 2752u);
        };
        auto issueWQ = [&](int pc) {
#pragma unroll
            for (int t = 0; t < 2; ++t) dma16(ldsb + (unsigned)(L_WQ + pc * 8192 + t * 1024) + (unsigned)ww * 2048u, voQ, rq, (unsigned)(pc * 128 + 16 * t) * 3072u);
        };
        const unsigned vW = mkvoW(lane);
        auto issueF2 = [&](int c) { issueFp(c, 2 * ww, vW, 0u); issueFp(c, 2 * ww + 1, vW, 0u); };
        FST(13);
        waitv_bar_e<12>(ep);
        STAMP(1);
        epoch = ep; mktok();
        issueX(2); issueKV(4); issueKV(5);
        waitv_bar<12>();
        FST(0);
        issueX(3); issueKV(6); issueKV(7);
        waitv_bar<12>();
        FST(1);
        issueW1(0); issueW1(1); issueW1(2); issueW1(3);
        waitv_bar<12>();
        FST(2);
        issueW1(4); issueW1(5); issueW1(6); issueW1(7);
        STAMP(2);
        waitv_bar<12>();
        FST(4);
        if (w == 4) *(LAS u32x4*)(L + L_GW + l16) = (u32x4){0x3f803f80u, 0x3f803f80u, 0x3f803f80u, 0x3f803f80u};
        issueWQ(0); issueWQ(1); issueWQ(2); issueWQ(3);
        {
            const unsigned sO = WS_A + (unsigned)((tb * NKS + NKS_H + hp * 2) * 1024);
            st16c(sentv, l16, rw, sO); st16c(sentv, l16, rw, sO + 1024u);
        }
        FST(5);
        waitv_bar<10>();
        FST(6);
        issueF2(0); issueF2(1); issueF2(2);
        FST(22);
        waitv_bar<6>();
        FST(23);
        issueF2(3); issueF2(4); issueF2(5); issueF2(6);
        unsigned voFK;
        { const int l = lane & 31, i = l < 24 ? l >> 1 : 0, kt = rb * 4 - 8 + i;
          voFK = WS_FK + (unsigned)((kt < 0 ? 0 : kt) * 16 + (hp & ~1) + (l & 1)) * 8u; }
        u32x2 fkv;
        ld8c(fkv, voFK, rw, 0u);
        FST(7);
        BAR_L();
        FST(8);
        waitv_tie2<0>(fkv);
        {
            unsigned spins = 0;
            while (!__all(fkv[0] == tok[0] && fkv[1] == tok[1]) && ++spins < SPIN_CAP) { ld8c(fkv, voFK, rw, 0u); waitv_tie2<0>(fkv); }
        }
        FST(24);
        {
            u32x4 kst[6], vst[6];
            const int hh = ww & 1;
#pragma unroll
            for (int t = 0; t < 6; ++t) {
                const int i = (ww >> 1) + 2 * t, kt = rb * 4 - 8 + i;
                const unsigned sst = (unsigned)(((hp * 64 + (kt < 0 ? 0 : kt)) * 2 + hh) * 1024);
                ld16c(kst[t], kt < 0 ? (unsigned)(lane & 32) * 16u : l16, rw, WS_K + sst); ld16c(vst[t], l16, rw, WS_V + sst);
            }
            sfor<0, 6>([&](auto T) {
                constexpr int t = decltype(T)::v;
                const int i = (ww >> 1) + 2 * t, kt = rb * 4 - 8 + i;
                waitv_tie<11 - 2 * t>(kst[t]);
                *(LAS u32x4*)(L + L_KST + (hh * 12 + i) * 1024 + l16) = kst[t];
                waitv_tie<10 - 2 * t>(vst[t]);
                *(LAS u32x4*)(L + L_VST + (hh * 12 + i) * 1024 + l16) = kt < 0 ? (u32x4){0u, 0u, 0u, 0u} : vst[t];
            });
        }
        STAMP(3);
        BAR_L();
    }
    STAMP(4);

    int lane2 = lane;
    asm volatile("" : "+v"(lane2));
    const unsigned vW = mkvoW(lane2), vO = mkvoO(lane2);
    const int fm = w & 1, kq = w >> 1;
    const unsigned sAf = WS_A + (unsigned)((rbF * 4 + rh * 2 + fm) * NKS + kq) * 1024u;
    u32x4 af[30];
    {
        const int nh = w >> 2, m = ww;
        const LAS char* kb = L + L_KST + (nh * 12 + m) * 1024 + l16;
        bf16x8 kf[9];
#pragma unroll
        for (int i = 0; i < 9; ++i) kf[i] = *(const LAS bf16x8*)(kb + ((i + 8) % 9) * 1024);
        if (!isC) qf = *(const LAS bf16x8*)(L + L_QX + m * 1024 + l16);
        const float g = ((const LAS float*)(L + L_GP))[m * 32 + r];
        const unsigned voFH = (unsigned)(rbF * 16 + (lane2 & 15)) * 8u;
        u32x2 fhv;
        ld8c(fhv, voFH, rw, WS_FH);
        const int t0 = tb >= 8 ? 0 : 8 - tb;
        const unsigned vstep = blk ? 0u : 1024u;
        const unsigned vb0 = ldsb + (blk ? (unsigned)(L_GW + (1 - h) * 128) : (unsigned)(L_VST + (nh * 12 + m) * 1024 + (4 * h + q) * 32 + p * 8));
        unsigned mk[16];
#pragma unroll
        for (int j = 0; j < 16; ++j) mk[j] = ((j & 3) + 8 * (j >> 2) + 4 * h <= r) ? 0xFFFFFFFFu : 0u;
        f32x16 zero;
#pragma unroll
        for (int j = 0; j < 16; ++j) zero[j] = 0.f;
        const float NEG = -INFINITY;
        auto score = [&](auto P, const f32x16& c) -> f32x16 { return MFMA32(kf[decltype(P)::v], qf, c); };
        auto merge = [&](const f32x16& a, const f32x16& b) {
            f32x16 o_;
#pragma unroll
            for (int j = 0; j < 16; ++j) { const float x_ = a[j], y_ = b[j]; o_[j] = __builtin_bit_cast(float, (__builtin_bit_cast(unsigned, x_) & mk[j]) | (__builtin_bit_cast(unsigned, y_) & ~mk[j])); }
            return o_;
        };
        float mx = NEG;
        auto red = [&](const f32x16& s_) {
#pragma unroll
            for (int j = 0; j < 16; j += 2) mx = fmaxf(fmaxf(s_[j], s_[j + 1]), mx);
        };
        {
            f32x16 S[4];
            S[0] = score(IC<0>{}, zero); S[1] = score(IC<1>{}, zero); S[2] = score(IC<2>{}, zero); S[3] = score(IC<3>{}, zero);
            SB();
            red(merge(S[0], S[1]));
            S[0] = score(IC<4>{}, zero); S[1] = score(IC<5>{}, zero);
            SB();
            sfor<2, 9>([&](auto P) {
                constexpr int pp = decltype(P)::v;
                red(S[pp % 4]);
                if constexpr (pp + 4 < 9) S[pp % 4] = score(IC<pp + 4>{}, zero);
                SB();
            });
        }
        {
            const u32x2 sw = __builtin_amdgcn_permlane32_swap(__builtin_bit_cast(unsigned, mx), __builtin_bit_cast(unsigned, mx), false, false);
            const unsigned s0_ = sw[0], s1_ = sw[1];
            mx = fmaxf(__builtin_bit_cast(float, s0_), __builtin_bit_cast(float, s1_));
        }
        f32x16 negm;
#pragma unroll
        for (int j = 0; j < 16; ++j) negm[j] = -mx;
        FST(9);
        f32x16 S[3];
        S[0] = score(IC<0>{}, negm); S[1] = score(IC<1>{}, negm); S[2] = score(IC<2>{}, negm);
        bf16x8 vf[2][2];
        auto rdv = [&](auto P, bf16x8 (&v)[2]) {
            constexpr int pp = decltype(P)::v, tile = (pp + 8) % 9;
            const unsigned dz = tile < t0 ? (unsigned)(L_ZERO - L_GW) : 0u;
            const LAS char* p0 = lp(vb0 + vstep * (unsigned)tile + (blk ? dz : 0u));
            v[0] = cat8(trd(p0), trd(p0 + 256)); v[1] = cat8(trd(p0 + 512), trd(p0 + 768));
        };
        rdv(IC<0>{}, vf[0]); rdv(IC<1>{}, vf[1]);
        if (isC) {
            waitv<1>();
            unsigned cnt = 0;
            if (lane == 0) cnt = __hip_atomic_fetch_add((LAS unsigned*)(L + L_CNT), 1u, __ATOMIC_RELAXED, __HIP_MEMORY_SCOPE_WORKGROUP);
            cnt = __builtin_amdgcn_readfirstlane(cnt);
            if (cnt == 3u && lane == 0) st8c(tok, 0u, rw, WS_FH + (unsigned)(rb * 16 + hp) * 8u);
        }
        FST(10);
        auto slot = [&](auto SL) {
            constexpr int sl = decltype(SL)::v;
            if constexpr (sl < 2) sfor<7 + 3 * sl, 10 + 3 * sl>([&](auto C) { issueFp(decltype(C)::v, w, vW, vO); });
            if constexpr (sl == FHS) {
                FST(11);
                waitv_tie2<0>(fhv);
                unsigned spins = 0;
                while (!__all(fhv[0] == tok[0] && fhv[1] == tok[1]) && ++spins < SPIN_CAP) { __builtin_amdgcn_s_sleep(1); ld8c(fhv, voFH, rw, WS_FH); waitv_tie2<0>(fhv); }
                FST(12);
            }
            if constexpr (sl >= FHS) {
                constexpr int ns = 8 - FHS, c0 = 22 * (sl - FHS) / ns, c1 = 22 * (sl - FHS + 1) / ns;
                sfor<c0, c1>([&](auto C) { constexpr int c = decltype(C)::v; ld16c(af[c], l16, rw, sAf + (unsigned)((c == 21 && kq >= 2) ? 0 : 4 * c) * 1024u); });
            }
        };
        auto expack = [&](const f32x16& s_, u32x4& p0, u32x4& p1) {
#pragma unroll
            for (int j = 0; j < 4; ++j) {
                p0[j] = cvtpk(__builtin_amdgcn_exp2f(s_[2 * j]), __builtin_amdgcn_exp2f(s_[2 * j + 1]));
                p1[j] = cvtpk(__builtin_amdgcn_exp2f(s_[8 + 2 * j]), __builtin_amdgcn_exp2f(s_[8 + 2 * j + 1]));
            }
        };
        f32x16 o;
        SB();
        {
            u32x4 p0, p1;
            expack(merge(S[0], S[1]), p0, p1);
            S[0] = score(IC<3>{}, negm); S[1] = score(IC<4>{}, negm);
            u32x4 d0, d1, e0, e1;
#pragma unroll
            for (int j = 0; j < 4; ++j) {
                const unsigned m0 = (mk[2 * j] & 0xFFFFu) | (mk[2 * j + 1] & 0xFFFF0000u), m1 = (mk[8 + 2 * j] & 0xFFFFu) | (mk[8 + 2 * j + 1] & 0xFFFF0000u);
                const unsigned a0 = p0[j], a1 = p1[j];
                d0[j] = a0 & m0; e0[j] = a0 & ~m0; d1[j] = a1 & m1; e1[j] = a1 & ~m1;
            }
            o = MFMA32(vf[0][0], __builtin_bit_cast(bf16x8, d0), zero);
            o = MFMA32(vf[0][1], __builtin_bit_cast(bf16x8, d1), o);
            rdv(IC<2>{}, vf[0]);
            o = MFMA32(vf[1][0], __builtin_bit_cast(bf16x8, e0), o);
            o = MFMA32(vf[1][1], __builtin_bit_cast(bf16x8, e1), o);
            slot(IC<0>{});
            SB();
        }
        sfor<2, 9>([&](auto P) {
            constexpr int pp = decltype(P)::v;
            if constexpr (pp + 1 < 9) rdv(IC<pp + 1>{}, vf[(pp + 1) & 1]);
            u32x4 p0, p1;
            expack(S[pp % 3], p0, p1);
            if constexpr (pp + 3 < 9) S[pp % 3] = score(IC<pp + 3>{}, negm);
            o = MFMA32(vf[pp & 1][0], __builtin_bit_cast(bf16x8, p0), o);
            o = MFMA32(vf[pp & 1][1], __builtin_bit_cast(bf16x8, p1), o);
            slot(IC<pp - 1>{});
            SB();
        });
        const float sc = g / o[8];
        st16c(nosent(pack8(o, 0, sc)), l16, rw, WS_A + (unsigned)((tb * NKS + NKS_H + hp * 2 + nh) * 1024));
    }
    STAMP(5);

    {
        const bool boss = blockIdx.x == 0 && w == 0;
        u32x4 ea[2];
        unsigned woff[2];
#pragma unroll
        for (int t = 0; t < 2; ++t) { const int row = 16 * kq + 4 * h + q; const int c16 = (t * 4 + blk * 2 + (p >> 1)) ^ (((row >> 1) & 1) << 2); woff[t] = (unsigned)(row * 128 + c16 * 16 + (p & 1) * 8); }
        f32x16 acc[2];
#pragma unroll
        for (int i = 0; i < 2; ++i)
#pragma unroll
            for (int j = 0; j < 16; ++j) acc[i][j] = 0.f;
        auto mm = [&](auto C) {
            constexpr int c = decltype(C)::v;
            if (c != 21 || kq < 2) {
                const LAS char* wb = L + fsl(c);
#pragma unroll
                for (int t = 0; t < 2; ++t) {
                    const bf16x8 wf = cat8(trd(wb + woff[t]), trd(wb + woff[t] + 8 * 128));
                    acc[t] = MFMA32(wf, __builtin_bit_cast(bf16x8, af[c]), acc[t]);
                }
            }
        };
        auto issueF1 = [&](auto C) { issueFp(decltype(C)::v, w, vW, vO); };
        waitv_bar<23>();
        STAMP(6);
        if (boss) { ld16c(ea[0], l16, rw, WS_FH); ld16c(ea[1], l16, rw, WS_FH + 1024u); }
        sfor<13, 19>(issueF1);
        sfor<0, 4>([&](auto C) { constexpr int c = decltype(C)::v; waitv_tie<28 - c>(af[c]); mm(C); });
        BAR_L(); sfor<19, 23>(issueF1);
        sfor<4, 8>([&](auto C) { constexpr int c = decltype(C)::v; waitv_tie<32 - c>(af[c]); mm(C); });
        BAR_L(); sfor<23, 27>(issueF1);
        sfor<8, 13>([&](auto C) { constexpr int c = decltype(C)::v; waitv_tie<36 - c>(af[c]); mm(C); });
        STAMP(7);
        waitv_bar<8>();
        sfor<13, 22>([&](auto C) { TIE(af[decltype(C)::v]); });
        sfor<27, 30>(issueF1);
        sfor<22, 30>([&](auto C) { constexpr int c = decltype(C)::v; ld16c(af[c], l16, rw, sAf + (unsigned)(NKS_H + 4 * (c - 22)) * 1024u); });
        bool epoch_done = !boss;
        if (boss) {
            TIE(ea[0]); TIE(ea[1]);
            bool ok = true;
#pragma unroll
            for (int j = 0; j < 2; ++j) { const unsigned a0 = ea[j][0], a1 = ea[j][1], a2 = ea[j][2], a3 = ea[j][3]; ok = ok && a0 == tok[0] && a1 == tok[1] && a2 == tok[0] && a3 == tok[1]; }
            if (__all(ok)) { epoch_done = true; if (lane == 0) { const unsigned long long e1 = epoch + 1; const u32x2 ev1 = {(unsigned)e1, (unsigned)(e1 >> 32)}; st8c(ev1, 0u, rw, WS_E); } }
        }
        sfor<13, 19>(mm);
        waitv_bar<15>();
        sfor<19, 22>(mm);
        STAMP(8);
        waitv<0>();
        sfor<22, 30>([&](auto C) { TIE(af[decltype(C)::v]); });
        {
            unsigned spins = 0;
            for (;;) {
                unsigned mxs = 0;
                sfor<22, 30>([&](auto C) { mxs = max(mxs, anysent(af[decltype(C)::v])); });
                if (__all(mxs != SENT) || ++spins >= SPIN_CAP) break;
                __builtin_amdgcn_s_sleep(1);
                sfor<22, 30>([&](auto C) { constexpr int c = decltype(C)::v; ld16c(af[c], l16, rw, sAf + (unsigned)(NKS_H + 4 * (c - 22)) * 1024u); });
                waitv<0>();
                sfor<22, 30>([&](auto C) { TIE(af[decltype(C)::v]); });
            }
        }
        STAMP(9);
        BAR_L();
        sfor<22, 30>(mm);
        BAR_L();
        STAMP(10);
        constexpr int RP = 272;
        {
            LAS char* dst = L + kq * (64 * RP) + (fm * 32 + r) * RP;
#pragma unroll
            for (int t = 0; t < 2; ++t)
#pragma unroll
                for (int g4 = 0; g4 < 4; ++g4)
                    *(LAS f32x4*)(dst + (t * 32 + g4 * 8 + 4 * h) * 4) = (f32x4){acc[t][4 * g4], acc[t][4 * g4 + 1], acc[t][4 * g4 + 2], acc[t][4 * g4 + 3]};
        }
        BAR_L();
        {
            const int tok_ = tid >> 3, oc = tid & 7;
            f32x4 a = {0.f, 0.f, 0.f, 0.f}, b = a;
#pragma unroll
            for (int k = 0; k < 4; ++k) {
                const LAS char* src = L + k * (64 * RP) + tok_ * RP + oc * 32;
                a += *(const LAS f32x4*)src; b += *(const LAS f32x4*)(src + 16);
            }
            u32x4 o = {cvtpk(a[0], a[1]), cvtpk(a[2], a[3]), cvtpk(b[0], b[1]), cvtpk(b[2], b[3])};
            *(GAS u32x4*)((char*)out + ((size_t)(rbF * 128 + rh * 64 + tok_) * 512 + ct * 64 + oc * 8) * 2) = o;
        }
        if (!epoch_done) {
            unsigned spins = 0;
            for (;;) {
                ld16c(ea[0], l16, rw, WS_FH); ld16c(ea[1], l16, rw, WS_FH + 1024u); waitv<0>(); TIE(ea[0]); TIE(ea[1]);
                bool ok = true;
#pragma unroll
                for (int j = 0; j < 2; ++j) { const unsigned a0 = ea[j][0], a1 = ea[j][1], a2 = ea[j][2], a3 = ea[j][3]; ok = ok && a0 == tok[0] && a1 == tok[1] && a2 == tok[0] && a3 == tok[1]; }
                if (__all(ok) || ++spins >= SPIN_CAP) break;
                __builtin_amdgcn_s_sleep(2);
            }
            if (lane == 0) { const unsigned long long e1 = epoch + 1; const u32x2 ev1 = {(unsigned)e1, (unsigned)(e1 >> 32)}; st8c(ev1, 0u, rw, WS_E); }
        }
    }
}

extern "C" void kernel_launch(void* const* d_in, const int* in_sizes, int n_in, void* d_out, int out_size, void* d_ws, size_t ws_size, hipStream_t stream) {
    (void)hipFuncSetAttribute((const void*)k_fused, hipFuncAttributeMaxDynamicSharedMemorySize, LDS_BYTES);
    k_fused<<<256, 512, LDS_BYTES, stream>>>(d_in[0], d_in[1], d_in[2], d_in[4], d_in[5], d_in[6], (char*)d_ws, d_out);
}
```

```cpp
#include <hip/hip_runtime.h>
#include <hip/hip_bf16.h>
#include <stdint.h>

#define DI __device__ __forceinline__
#define LAS __attribute__((address_space(3)))
#define GAS __attribute__((address_space(1)))
typedef short bf16x8 __attribute__((ext_vector_type(8)));
typedef short s16x4 __attribute__((ext_vector_type(4)));
typedef float f32x16 __attribute__((ext_vector_type(16)));
typedef float f32x4 __attribute__((ext_vector_type(4)));
typedef float f32x2 __attribute__((ext_vector_type(2)));
typedef __bf16 bf16x2_t __attribute__((ext_vector_type(2)));
typedef unsigned u32x4 __attribute__((ext_vector_type(4)));
typedef unsigned u32x2 __attribute__((ext_vector_type(2)));
typedef __amdgpu_buffer_rsrc_t rsrc_t;

constexpr int S_ = 2048, D_ = 512, DFF = 1376;
constexpr int NKS_H = DFF / 16;
constexpr int NKS = NKS_H + 32;
constexpr unsigned WS_E = 0;
constexpr unsigned WS_FH = 8192;
constexpr unsigned WS_FK = 16384;
constexpr unsigned WS_K = 1u << 20;
constexpr unsigned WS_V = WS_K + (2u << 20);
constexpr unsigned WS_A = WS_V + (2u << 20);
constexpr unsigned WS_END = WS_A + 64u * NKS * 1024u;
constexpr unsigned WS_DBG = WS_END;
constexpr unsigned SENT = 0xFFFFFFFFu;
constexpr unsigned SPIN_CAP = 1u << 18;

constexpr float QSCALE = 0.25f * 1.4426950408889634f;

#define MFMA32(a, b, c) __builtin_amdgcn_mfma_f32_32x32x16_bf16((a), (b), (c), 0, 0, 0)

DI unsigned cvtpk(float lo, float hi) { f32x2 v = {lo, hi}; bf16x2_t b = __builtin_convertvector(v, bf16x2_t); return __builtin_bit_cast(unsigned, b); }
DI u32x4 pack8(const f32x16& x, int s, float sc) {
    u32x4 p;
    p[0] = cvtpk(x[8 * s + 0] * sc, x[8 * s + 1] * sc); p[1] = cvtpk(x[8 * s + 2] * sc, x[8 * s + 3] * sc);
    p[2] = cvtpk(x[8 * s + 4] * sc, x[8 * s + 5] * sc); p[3] = cvtpk(x[8 * s + 6] * sc, x[8 * s + 7] * sc);
    return p;
}
DI u32x4 nosent(u32x4 p) {
#pragma unroll
    for (int j = 0; j < 4; ++j) { const unsigned v = p[j]; p[j] = v == SENT ? 0x7FC07FC0u : v; }
    return p;
}
DI void dma16(unsigned lds_dst, unsigned voff, rsrc_t rs, unsigned soff) {
    asm volatile("s_mov_b32 m0, %0\n\ts_nop 0\n\tbuffer_load_dwordx4 %1, %2, %3 offen lds" ::"s"(lds_dst), "v"(voff), "s"(rs), "s"(soff) : "memory");
}
DI void ld16c(u32x4& v, unsigned voff, rsrc_t rs, unsigned soff) {
    asm volatile("buffer_load_dwordx4 %0, %1, %2, %3 offen sc1" : "=&v"(v) : "v"(voff), "s"(rs), "s"(soff) : "memory");
}
DI void ld8c(u32x2& v, unsigned voff, rsrc_t rs, unsigned soff) {
    asm volatile("buffer_load_dwordx2 %0, %1, %2, %3 offen sc1" : "=&v"(v) : "v"(voff), "s"(rs), "s"(soff) : "memory");
}
DI void st16c(u32x4 v, unsigned voff, rsrc_t rs, unsigned soff) {
    asm volatile("buffer_store_dwordx4 %0, %1, %2, %3 offen sc1\n\ts_nop 1" ::"v"(v), "v"(voff), "s"(rs), "s"(soff) : "memory");
}
DI void st8c(u32x2 v, unsigned voff, rsrc_t rs, unsigned soff) {
    asm volatile("buffer_store_dwordx2 %0, %1, %2, %3 offen sc1" ::"v"(v), "v"(voff), "s"(rs), "s"(soff) : "memory");
}
template <int N> DI void waitv() { asm volatile("s_waitcnt vmcnt(%0)" ::"n"(N) : "memory"); }
template <int N> DI void waitv_bar() { asm volatile("s_waitcnt vmcnt(%0) lgkmcnt(0)\n\ts_barrier" ::"n"(N) : "memory"); }
DI void ld_epoch(unsigned long long& e, const char* ws) { asm volatile("s_load_dwordx2 %0, %1, 0x0" : "=s"(e) : "s"(ws) : "memory"); }
template <int N> DI void waitv_bar_e(unsigned long long& e) { asm volatile("s_waitcnt vmcnt(%1) lgkmcnt(0)\n\ts_barrier" : "+s"(e) : "n"(N) : "memory"); }
template <int N> DI void waitv_tie(u32x4& a) { asm volatile("s_waitcnt vmcnt(%1)" : "+v"(a) : "n"(N) : "memory"); }
template <int N> DI void waitv_tie2(u32x2& a) { asm volatile("s_waitcnt vmcnt(%1)" : "+v"(a) : "n"(N) : "memory"); }
DI void TIE(u32x4& x) { asm volatile("" : "+v"(x)); }
DI void TIE(u32x2& x) { asm volatile("" : "+v"(x)); }
#define BAR_L() asm volatile("s_waitcnt lgkmcnt(0)\n\ts_barrier" ::: "memory")
DI s16x4 trd(const LAS char* p) { return __builtin_bit_cast(s16x4, __builtin_amdgcn_ds_read_tr16_b64_v4i16((LAS s16x4*)p)); }
DI bf16x8 cat8(s16x4 lo, s16x4 hi) { return __builtin_shufflevector(lo, hi, 0, 1, 2, 3, 4, 5, 6, 7); }
DI rsrc_t mkrs(const void* p, unsigned bytes) { return __builtin_amdgcn_make_buffer_rsrc((void*)p, 0, (int)bytes, 0x00020000); }
DI unsigned umax3(unsigned a, unsigned b, unsigned c) { return max(max(a, b), c); }
DI unsigned anysent(const u32x4& a) { const unsigned a0 = a[0], a1 = a[1], a2 = a[2], a3 = a[3]; return max(max(a0, a1), max(a2, a3)); }

#define STAMP(i) do { } while (0)
#define STAMPC(i) do { } while (0)
#define STAMPW 0
#define FST(i) do { } while (0)

template <int I> struct IC { static constexpr int v = I; };
template <int B, int E, class F> DI void sfor(F&& f) { if constexpr (B < E) { f(IC<B>{}); sfor<B + 1, E>(f); } }

DI void block_map(int b, int& rb, int& hp) { const int xg = b & 7, idx = b >> 3; rb = (xg >> 2) * 8 + (idx >> 2); hp = (xg & 3) * 4 + (idx & 3); }

constexpr int KB = 1024;
__host__ __device__ constexpr int stS(int g) { return (g == 3 ? 0 : g) * 48 * KB; }
constexpr int L_W1 = 48 * KB;
constexpr int L_WQ = 0;
__host__ __device__ constexpr int fsl(int c) {
    constexpr int t[19] = {32, 40, 144, 48, 56, 64, 72, 128, 136, 0, 8, 16, 24, 80, 88, 96, 104, 112, 120};
    return t[c % 19] * KB;
}
constexpr int L_KST = 80 * KB, L_VST = 104 * KB;
constexpr int L_MISC = 152 * KB;
constexpr int L_GW = L_MISC, L_GP = L_MISC + 1024, L_QX = L_MISC + 2048, L_ONE = L_MISC + 6144, L_CNT = L_MISC + 6400, L_ZERO = L_MISC + 6656, L_DUM = L_MISC + 7680;
constexpr int LDS_BYTES = 163840;
#define KVFLAG 16
#define FHS 2

#define PFC 6
#define PFCW 7
#define ROT (((idx & 3) + (idx >> 2)) & 3)
#define NPF 1
#define PFAW 4
#define PFB 2
#define PFWC 0
#define BAR_N(n) asm volatile("s_waitcnt lgkmcnt(" #n ")\n\ts_barrier" ::: "memory")
#define SB() __builtin_amdgcn_sched_barrier(0)
typedef short s16x2 __attribute__((ext_vector_type(2)));
DI unsigned relu2(unsigned v) { const s16x2 z = {0, 0}; return __builtin_bit_cast(unsigned, __builtin_elementwise_max(__builtin_bit_cast(s16x2, v), z)); }
DI const LAS char* lp(unsigned a) { return (const LAS char*)(uintptr_t)a; }
DI unsigned opq(unsigned v) { asm volatile("" : "+v"(v)); return v; }

__global__ __launch_bounds__(512, 2) void k_fused(const void* x, const void* wqkv, const void* wo, const void* w1, const void* w2, const void* gw, char* ws, void* out) {
    extern __shared__ __attribute__((aligned(16))) char smem[];
    LAS char* L = (LAS char*)smem;
    const int tid = threadIdx.x, lane = tid & 63;
    int w = __builtin_amdgcn_readfirstlane(tid >> 6);
    asm volatile("" : "+s"(w));
    const bool isC = w < 4;
    const int ww = w & 3, r = lane & 31, h = lane >> 5;
    int rb, hp; block_map(blockIdx.x, rb, hp);
    const int xg = blockIdx.x & 7, idx = blockIdx.x >> 3;
    const int rbF = 2 * xg + (idx >> 4), rh = (idx >> 3) & 1, ct = idx & 7;
    const unsigned ldsb = (unsigned)(uintptr_t)L;
    auto touch = [&](const char* a) { asm volatile("s_mov_b32 m0, %0\n\ts_nop 0\n\tglobal_load_lds_dword %1, off" ::"s"(ldsb + (unsigned)L_DUM), "v"(a) : "memory"); };
    asm volatile(".Lpf_start:");
    STAMP(0); STAMPC(12);
    if (w == PFCW) {
        unsigned long long pc0; unsigned clen, cback;
        asm volatile("s_getpc_b64 %0\n.Lpf_here:\n\ts_mov_b32 %1, .Lpf_end-.Lpf_start-4\n\ts_mov_b32 %2, .Lpf_here-.Lpf_start" : "=s"(pc0), "=s"(clen), "=s"(cback));
        const unsigned oc = min((unsigned)((idx * PFC + (lane < PFC ? lane : 0)) * 128), clen) & ~3u;
        const char* a = (const char*)(pc0 - cback) + oc;
        touch(a);
    }
    unsigned long long epoch;
    const rsrc_t rx = mkrs(x, S_ * D_ * 2), rq = mkrs(wqkv, D_ * 3 * D_ * 2), r1 = mkrs(w1, D_ * DFF * 2), rg = mkrs(gw, D_ * 2);
    const rsrc_t r2 = mkrs(w2, DFF * D_ * 2), ro = mkrs(wo, D_ * D_ * 2), rw = mkrs(ws, WS_END);
    const unsigned l16 = (unsigned)lane * 16u;
    const int tb = rb * 4 + ww;
    const int q = (lane & 15) >> 2, p = lane & 3, blk = (lane >> 4) & 1;
    const int u0 = (hp * 3 < 40) ? hp * 3 : 40;
    const int row8 = lane >> 3, pos = lane & 7;
    const u32x4 sentv = {SENT, SENT, SENT, SENT};

    const unsigned voX = (unsigned)((8 * ww + row8) * 1024 + (pos ^ (((8 * ww + row8) >> 1) & 7)) * 16);
    const unsigned sX = (unsigned)rb * 128u * 1024u;
    const int rot = ROT;
    auto issueX = [&](int g) {
#pragma unroll
        for (int t = 0; t < 8; ++t)
            dma16(ldsb + (unsigned)(g * 48 * KB + (t >> 2) * 16384 + (t & 3) * 4096) + (unsigned)ww * 1024u, voX, rx, sX + (unsigned)(((g + rot) & 3) * 256 + (t & 3) * 32768 + (t >> 2) * 128));
    };
    auto issueX3 = [&]() {
#pragma unroll
        for (int t = 0; t < 8; ++t)
            dma16(ldsb + (unsigned)((t >> 2) * 16384 + (t & 3) * 4096) + (unsigned)ww * 1024u, voX, rx, sX + (unsigned)(((3 + rot) & 3) * 256 + (t & 3) * 32768 + (t >> 2) * 128));
    };
    const unsigned voKV = (unsigned)((16 * ww + row8) * 3072 + 1024 + (hp & 1) * 1024 + (hp >> 1) * 128 + (pos ^ (((row8 >> 1) & 1) << 2)) * 16);
    auto issueKV = [&](int j) {
#pragma unroll
        for (int t = 0; t < 2; ++t) dma16(ldsb + (unsigned)(((j >> 1) == 3 ? 0 : (j >> 1)) * 48 * KB + 32 * KB + (j & 1) * 8192 + t * 1024) + (unsigned)ww * 2048u, voKV, rq, (unsigned)(((j + 2 * rot) & 7) * 64 + 8 * t) * 3072u);
    };
    auto mkvoW = [&](int ln) { const int r8 = ln >> 3, ps = ln & 7; return (unsigned)(r8 * 1024 + ct * 128 + (ps ^ (((r8 >> 1) & 1) << 2)) * 16); };
    auto mkvoO = [&](int ln) { const int r8 = ln >> 3, ps = ln & 7; return (unsigned)((8 * (r8 >> 2) + (r8 & 3)) * 1024 + ct * 128 + (ps ^ (((r8 >> 1) & 1) << 2)) * 16); };
    auto issueFp = [&](int c, int pi, unsigned vW, unsigned vO) {
        const unsigned dst = ldsb + (unsigned)fsl(c) + (unsigned)pi * 1024u;
        if (c < 22) dma16(dst, vW, r2, (unsigned)(c * 65536) + (unsigned)pi * 8192u);
        else dma16(dst, vO, ro, (unsigned)((c - 22) * 65536) + (unsigned)(16 * (pi >> 1) + 4 * (pi & 1)) * 1024u);
    };

    unsigned long long tok64 = 0;
    u32x2 tok;
    bf16x8 qf;
    auto mktok = [&]() {
        tok64 = epoch * 0x9E3779B97F4A7C15ull + 0x632BE59BD9B4E019ull;
        tok = (u32x2){(unsigned)tok64, (unsigned)(tok64 >> 32)};
    };

    const int hc = isC ? 0 : 1;
    auto issueHalf = [&](int g) {
        issueKV(2 * g + hc);
#pragma unroll
        for (int t = 0; t < 4; ++t)
            dma16(ldsb + (unsigned)(g * 48 * KB + t * 4096) + (unsigned)(hc * 16384 + ww * 1024), voX, rx, sX + (unsigned)(((g + rot) & 3) * 256 + t * 32768) + (unsigned)hc * 128u);
    };
    auto pfAddrA = [&]() -> const char* {
        const int g4 = xg & 3, r0 = 8 * ((xg >> 2) * 32 + idx) + 2 * ww, l = lane < 18 ? lane : 0;
        const int rr = l >= 7 ? 1 : 0, c = l - 7 * rr;
        const unsigned o1 = min((unsigned)((((r0 + rr) * 2752 + g4 * 768) & ~127) + c * 128), (unsigned)(D_ * DFF * 2 - 4));
        const unsigned oq = (unsigned)((r0 + ((l >> 1) & 1)) * 3072 + g4 * 256 + (l & 1) * 128);
        return l < 14 ? (const char*)w1 + o1 : (const char*)wqkv + oq;
    };
    auto pfAddrB = [&]() -> const char* {
        const int l = (lane & 31) < 15 ? (lane & 31) : 0;
        return l < 4 ? (const char*)wo + (unsigned)((blockIdx.x * 16 + ww * 4 + l) * 128) : (const char*)w2 + (unsigned)(min((int)(blockIdx.x * 43 + ww * 11 + (l - 4)), (int)(blockIdx.x * 43 + 42)) * 128);
    };
    auto pfA = [&]() { touch((PFB == 0 && lane >= 32) ? pfAddrB() : pfAddrA()); };
    auto pfB = [&]() { touch(pfAddrB()); };
    if (w == 0) dma16(ldsb + L_GW, (l16 + (unsigned)rot * 256u) & 1023u, rg, 0u);
    issueHalf(0);
    if (PFAW < 4 ? isC : !isC) pfA();
    issueHalf(1);
    FST(15);
    if (isC) {
        const unsigned sKV = ((hp & 1) ? WS_V : WS_K) + (unsigned)(((hp & ~1) * 64 + tb) * 2048);
        unsigned long long ep; ld_epoch(ep, ws);
        const int xrow = ww * 32 + r;
        unsigned xoff[4];
#pragma unroll
        for (int s = 0; s < 4; ++s) { const int c = 2 * s + h; xoff[s] = ldsb + (unsigned)(xrow * 128 + ((c ^ ((xrow >> 1) & 7)) * 16)); }
        const unsigned kvoff = (unsigned)((8 * h + q) * 128 + ((blk * 2 + (p >> 1)) ^ (((q >> 1) & 1) << 2)) * 16 + (p & 1) * 8);
        const unsigned gwoff = ldsb + (((lane & 15) == blk) ? (unsigned)(L_GW + h * 16) : (unsigned)L_ZERO);
        bf16x8 xf[32];
        f32x16 acc[2];
#pragma unroll
        for (int j = 0; j < 16; ++j) { acc[0][j] = 0.f; acc[1][j] = 0.f; }
        f32x4 ag = {0.f, 0.f, 0.f, 0.f};
        FST(13);
        waitv_bar_e<(PFAW < 4 ? 6 + NPF : 6)>(ep);
        STAMP(1);
        epoch = ep; mktok();
        sfor<0, 4>([&](auto G) {
            constexpr int g = decltype(G)::v;
            const unsigned hi = stS(g) >= 65536 ? 65536u : 0u;
#pragma unroll
            for (int c = 0; c < 2; ++c)
#pragma unroll
                for (int s = 0; s < 4; ++s) xf[g * 8 + c * 4 + s] = *(const LAS bf16x8*)(lp((stS(g) >= 65536 ? opq(xoff[s] + hi) : xoff[s])) + (stS(g) - (int)hi + c * 16384));
            const unsigned kb0 = opq(ldsb + kvoff + (unsigned)(stS(g) + 32 * KB)), kb1 = opq(ldsb + (kvoff ^ 64u) + (unsigned)(stS(g) + 32 * KB));
            constexpr int DK = 4;
            bf16x8 wr[DK + 1], gf[4];
            sfor<0, 16 + DK>([&](auto I) {
                constexpr int i = decltype(I)::v;
                if constexpr (i < 16) {
                    const LAS char* pw = lp((i & 1) ? kb1 : kb0) + (i >> 3) * 8192 + ((i >> 1) & 3) * 2048;
                    wr[i % (DK + 1)] = cat8(trd(pw), trd(pw + 512));
                    if constexpr ((i & 1) == 0) gf[(i >> 1) & 3] = *(const LAS bf16x8*)(lp(gwoff) + (g * 128 + (i >> 1) * 16) * 2);
                }
                if constexpr (i >= DK) {
                    constexpr int j = i - DK;
                    acc[j & 1] = MFMA32(wr[j % (DK + 1)], xf[g * 8 + (j >> 1)], acc[j & 1]);
                    if constexpr ((j & 1) == 1) ag = __builtin_amdgcn_mfma_f32_16x16x32_bf16(gf[(j >> 1) & 3], xf[g * 8 + (j >> 1)], ag, 0, 0, 0);
                }
                SB();
            });
            FST(g);
            if constexpr (g == 0) waitv_bar<0>(); else if constexpr (g < 3) BAR_L();
        });
        STAMP(2);
        if (lane < 16) {
            ((LAS float*)(L + L_GP))[ww * 32 + lane] = 1.f / (1.f + __expf(-ag[0]));
            ((LAS float*)(L + L_GP))[ww * 32 + 16 + lane] = 1.f / (1.f + __expf(-ag[1]));
        }
        const float og = 1.f - ((const LAS float*)(L + L_GP))[xrow];
        const unsigned ql = (hp & 1) ? (unsigned)(2 * r + h) * 16u : l16;
        const unsigned w1off = ldsb + (unsigned)((8 * h + q) * 192 + blk * 32 + p * 8);
        const unsigned w1a = opq(w1off + (unsigned)L_W1), w1b = opq(w1off + (unsigned)(L_W1 + 60 * KB));
        const unsigned wqo = opq(ldsb + (unsigned)(L_WQ + (8 * h + q) * 64 + blk * 32 + p * 8));
        const unsigned sA = WS_A + (unsigned)(tb * NKS + 2 * u0) * 1024u;
        const int dup = hp * 3 - u0;
        BAR_L();
        FST(4);
        {
            constexpr int DP = 6, RP_ = 8;
            bf16x8 wr[RP_];
            f32x16 ah[2];
            u32x4 pk;
            sfor<0, 128 + DP>([&](auto I) {
                constexpr int i = decltype(I)::v;
                if constexpr (i == 16) { FST(5); BAR_N(12); FST(6); }
                if constexpr (i == 84) { FST(22); BAR_N(12); FST(23); }
                if constexpr (i == 96) { FST(7); BAR_N(4); FST(8); }
                if constexpr (i < 96) {
                    constexpr int t = i / 32, c = (i % 32) / 4, s = i % 4;
                    const LAS char* pw = lp(c < 5 ? w1a : w1b) + ((c < 5 ? c : c - 5) * 12 * KB + s * 16 * 192 + t * 64);
                    wr[i % RP_] = cat8(trd(pw), trd(pw + 4 * 192));
                } else if constexpr (i < 128) {
                    const LAS char* pw = lp(wqo) + (i - 96) * 1024;
                    wr[i % RP_] = cat8(trd(pw), trd(pw + 256));
                }
                if constexpr (i >= DP) {
                    constexpr int j = i - DP, T = j / 32, jj = j % 32;
                    if constexpr (jj == 0) {
                        f32x16 z;
#pragma unroll
                        for (int e = 0; e < 16; ++e) z[e] = 0.f;
                        ah[T & 1] = MFMA32(wr[j % RP_], xf[jj], z);
                    } else ah[T & 1] = MFMA32(wr[j % RP_], xf[jj], ah[T & 1]);
                    if constexpr (T == 0 && jj < 16) {
                        constexpr int n = jj / 4, qd = jj % 4, t = n >> 1, s = n & 1;
                        pk[qd] = cvtpk(acc[t][8 * s + 2 * qd], acc[t][8 * s + 2 * qd + 1]);
                        if constexpr (qd == 3) st16c(pk, ql, rw, sKV + (unsigned)(t * 131072 + s * 1024));
                    }
                    if constexpr (T == 1 && jj == KVFLAG) {
                        FST(20);
                        waitv<(KVFLAG >= 9) ? 2 : (KVFLAG >= 5) ? 1 : 0>();
                        FST(21);
                        if (lane == 0) st8c(tok, 0u, rw, WS_FK + (unsigned)(tb * 16 + hp) * 8u);
                    }
                    if constexpr (T >= 1 && jj >= 1 && jj < 9) {
                        constexpr int n = (jj - 1) / 4, qd = (jj - 1) % 4, t = T - 1;
                        pk[qd] = relu2(cvtpk(ah[t & 1][8 * n + 2 * qd] * og, ah[t & 1][8 * n + 2 * qd + 1] * og));
                        if constexpr (qd == 3) { if (t == 0 || t >= dup) st16c(pk, l16, rw, sA + (unsigned)((2 * t + n) * 1024)); }
                    }
                }
                SB();
            });
            qf = __builtin_bit_cast(bf16x8, pack8(ah[1], 0, QSCALE));
            *(LAS u32x4*)(L + L_QX + ww * 1024 + l16) = pack8(ah[1], 1, QSCALE);
        }
        STAMP(3);
        BAR_L();
    } else {
        unsigned long long ep; ld_epoch(ep, ws);
        if (w == 5 && lane == 0) *(LAS unsigned*)(L + L_CNT) = 0u;
        if (w == 6) *(LAS u32x4*)(L + L_ZERO + l16) = (u32x4){0u, 0u, 0u, 0u};
        unsigned voW1[3];
#pragma unroll
        for (int t = 0; t < 3; ++t) { const int li = 64 * t + lane, row = li / 12, c = li - row * 12; voW1[t] = (unsigned)((16 * ww + row) * 2752 + u0 * 64 + c * 16); }
        const unsigned voQ = (unsigned)((32 * ww + (lane >> 2)) * 3072 + hp * 64 + (lane & 3) * 16);
        auto issueW1 = [&](int c) {
#pragma unroll
            for (int t = 0; t < 3; ++t) dma16(ldsb + (unsigned)(L_W1 + c * 12 * KB + t * 1024) + (unsigned)ww * 3072u, voW1[t], r1, (unsigned)((c + 2 * rot) & 7) * 64u * 2752u);
        };
        auto issueWQ = [&](int pc) {
#pragma unroll
            for (int t = 0; t < 2; ++t) dma16(ldsb + (unsigned)(L_WQ + pc * 8192 + t * 1024) + (unsigned)ww * 2048u, voQ, rq, (unsigned)(((pc + rot) & 3) * 128 + 16 * t) * 3072u);
        };
        const unsigned vW = mkvoW(lane);
        auto issueF2 = [&](int c) { issueFp(c, 2 * ww, vW, 0u); issueFp(c, 2 * ww + 1, vW, 0u); };
        FST(13);
        waitv_bar_e<(PFAW >= 4 ? 6 + NPF : 6)>(ep);
        STAMP(1);
        epoch = ep; mktok();
        issueX(2); issueKV(4); issueKV(5);
        if (PFB == 1) pfB();
        waitv_bar<(PFB == 1 ? 12 + NPF : 12)>();
        FST(0);
        issueX3(); issueKV(6); issueKV(7);
        if (PFB == 2) pfB();
        waitv_bar<(PFB == 2 ? 12 + NPF : 12)>();
        FST(1);
        issueW1(0); issueW1(1); issueW1(2); issueW1(3);
        if (PFB == 3) pfB();
        waitv_bar<(PFB == 3 ? 12 + NPF : 12)>();
        FST(2);
        issueW1(4); issueW1(5); issueW1(6); issueW1(7);
        STAMP(2);
        waitv_bar<12>();
        FST(4);
        if (w == 4) *(LAS u32x4*)(L + L_GW + l16) = (u32x4){0x3f803f80u, 0x3f803f80u, 0x3f803f80u, 0x3f803f80u};
        issueWQ(0); issueWQ(1); issueWQ(2); issueWQ(3);
        {
            const unsigned sO = WS_A + (unsigned)((tb * NKS + NKS_H + hp * 2) * 1024);
            st16c(sentv, l16, rw, sO); st16c(sentv, l16, rw, sO + 1024u);
        }
        FST(5);
        waitv_bar<10>();
        FST(6);
        issueF2(0); issueF2(1); issueF2(2);
        FST(22);
        waitv_bar<6>();
        FST(23);
        issueF2(3); issueF2(4); issueF2(5); issueF2(6);
        unsigned voFK;
        { const int l = lane & 31, i = l < 24 ? l >> 1 : 0, kt = rb * 4 - 8 + i;
          voFK = WS_FK + (unsigned)((kt < 0 ? 0 : kt) * 16 + (hp & ~1) + (l & 1)) * 8u; }
        u32x2 fkv;
        ld8c(fkv, voFK, rw, 0u);
        FST(7);
        BAR_L();
        FST(8);
        waitv_tie2<0>(fkv);
        {
            unsigned spins = 0;
            while (!__all(fkv[0] == tok[0] && fkv[1] == tok[1]) && ++spins < SPIN_CAP) { ld8c(fkv, voFK, rw, 0u); waitv_tie2<0>(fkv); }
        }
        FST(24);
        {
            u32x4 kst[6], vst[6];
            const int hh = ww & 1;
#pragma unroll
            for (int t = 0; t < 6; ++t) {
                const int i = (ww >> 1) + 2 * t, kt = rb * 4 - 8 + i;
                const unsigned sst = (unsigned)(((hp * 64 + (kt < 0 ? 0 : kt)) * 2 + hh) * 1024);
                ld16c(kst[t], kt < 0 ? (unsigned)(lane & 32) * 16u : l16, rw, WS_K + sst); ld16c(vst[t], l16, rw, WS_V + sst);
            }
            sfor<0, 6>([&](auto T) {
                constexpr int t = decltype(T)::v;
                const int i = (ww >> 1) + 2 * t, kt = rb * 4 - 8 + i;
                waitv_tie<11 - 2 * t>(kst[t]);
                *(LAS u32x4*)(L + L_KST + (hh * 12 + i) * 1024 + l16) = kst[t];
                waitv_tie<10 - 2 * t>(vst[t]);
                *(LAS u32x4*)(L + L_VST + (hh * 12 + i) * 1024 + l16) = kt < 0 ? (u32x4){0u, 0u, 0u, 0u} : vst[t];
            });
        }
        STAMP(3);
        BAR_L();
    }
    STAMP(4);

    int lane2 = lane;
    asm volatile("" : "+v"(lane2));
    const unsigned vW = mkvoW(lane2), vO = mkvoO(lane2);
    const int fm = w & 1, kq = w >> 1;
    const unsigned sAf = WS_A + (unsigned)((rbF * 4 + rh * 2 + fm) * NKS + kq) * 1024u;
    u32x4 af[30];
    {
        const int nh = w >> 2, m = ww;
        const LAS char* kb = L + L_KST + (nh * 12 + m) * 1024 + l16;
        bf16x8 kf[9];
#pragma unroll
        for (int i = 0; i < 9; ++i) kf[i] = *(const LAS bf16x8*)(kb + ((i + 8) % 9) * 1024);
        if (!isC) qf = *(const LAS bf16x8*)(L + L_QX + m * 1024 + l16);
        const float g = ((const LAS float*)(L + L_GP))[m * 32 + r];
        const unsigned voFH = (unsigned)(rbF * 16 + (lane2 & 15)) * 8u;
        u32x2 fhv;
        ld8c(fhv, voFH, rw, WS_FH);
        const int t0 = tb >= 8 ? 0 : 8 - tb;
        const unsigned vstep = blk ? 0u : 1024u;
        const unsigned vb0 = ldsb + (blk ? (unsigned)(L_GW + (1 - h) * 128) : (unsigned)(L_VST + (nh * 12 + m) * 1024 + (4 * h + q) * 32 + p * 8));
        unsigned mk[16];
#pragma unroll
        for (int j = 0; j < 16; ++j) mk[j] = ((j & 3) + 8 * (j >> 2) + 4 * h <= r) ? 0xFFFFFFFFu : 0u;
        f32x16 zero;
#pragma unroll
        for (int j = 0; j < 16; ++j) zero[j] = 0.f;
        const float NEG = -INFINITY;
        auto score = [&](auto P, const f32x16& c) -> f32x16 { return MFMA32(kf[decltype(P)::v], qf, c); };
        auto merge = [&](const f32x16& a, const f32x16& b) {
            f32x16 o_;
#pragma unroll
            for (int j = 0; j < 16; ++j) { const float x_ = a[j], y_ = b[j]; o_[j] = __builtin_bit_cast(float, (__builtin_bit_cast(unsigned, x_) & mk[j]) | (__builtin_bit_cast(unsigned, y_) & ~mk[j])); }
            return o_;
        };
        float mx = NEG;
        auto red = [&](const f32x16& s_) {
#pragma unroll
            for (int j = 0; j < 16; j += 2) mx = fmaxf(fmaxf(s_[j], s_[j + 1]), mx);
        };
        {
            f32x16 S[4];
            S[0] = score(IC<0>{}, zero); S[1] = score(IC<1>{}, zero); S[2] = score(IC<2>{}, zero); S[3] = score(IC<3>{}, zero);
            SB();
            red(merge(S[0], S[1]));
            S[0] = score(IC<4>{}, zero); S[1] = score(IC<5>{}, zero);
            SB();
            sfor<2, 9>([&](auto P) {
                constexpr int pp = decltype(P)::v;
                red(S[pp % 4]);
                if constexpr (pp + 4 < 9) S[pp % 4] = score(IC<pp + 4>{}, zero);
                SB();
            });
        }
        {
            const u32x2 sw = __builtin_amdgcn_permlane32_swap(__builtin_bit_cast(unsigned, mx), __builtin_bit_cast(unsigned, mx), false, false);
            const unsigned s0_ = sw[0], s1_ = sw[1];
            mx = fmaxf(__builtin_bit_cast(float, s0_), __builtin_bit_cast(float, s1_));
        }
        f32x16 negm;
#pragma unroll
        for (int j = 0; j < 16; ++j) negm[j] = -mx;
        FST(9);
        f32x16 S[3];
        S[0] = score(IC<0>{}, negm); S[1] = score(IC<1>{}, negm); S[2] = score(IC<2>{}, negm);
        bf16x8 vf[2][2];
        auto rdv = [&](auto P, bf16x8 (&v)[2]) {
            constexpr int pp = decltype(P)::v, tile = (pp + 8) % 9;
            const unsigned dz = tile < t0 ? (unsigned)(L_ZERO - L_GW) : 0u;
            const LAS char* p0 = lp(vb0 + vstep * (unsigned)tile + (blk ? dz : 0u));
            v[0] = cat8(trd(p0), trd(p0 + 256)); v[1] = cat8(trd(p0 + 512), trd(p0 + 768));
        };
        rdv(IC<0>{}, vf[0]); rdv(IC<1>{}, vf[1]);
        if (isC) {
            waitv<1>();
            unsigned cnt = 0;
            if (lane == 0) cnt = __hip_atomic_fetch_add((LAS unsigned*)(L + L_CNT), 1u, __ATOMIC_RELAXED, __HIP_MEMORY_SCOPE_WORKGROUP);
            cnt = __builtin_amdgcn_readfirstlane(cnt);
            if (cnt == 3u && lane == 0) st8c(tok, 0u, rw, WS_FH + (unsigned)(rb * 16 + hp) * 8u);
        }
        FST(10);
        auto slot = [&](auto SL) {
            constexpr int sl = decltype(SL)::v;
            if constexpr (sl < 2) sfor<7 + 3 * sl, 10 + 3 * sl>([&](auto C) { issueFp(decltype(C)::v, w, vW, vO); });
            if constexpr (sl == FHS) {
                FST(11);
                waitv_tie2<0>(fhv);
                unsigned spins = 0;
                while (!__all(fhv[0] == tok[0] && fhv[1] == tok[1]) && ++spins < SPIN_CAP) { __builtin_amdgcn_s_sleep(1); ld8c(fhv, voFH, rw, WS_FH); waitv_tie2<0>(fhv); }
                FST(12);
            }
            if constexpr (sl >= FHS) {
                constexpr int ns = 8 - FHS, c0 = 22 * (sl - FHS) / ns, c1 = 22 * (sl - FHS + 1) / ns;
                sfor<c0, c1>([&](auto C) { constexpr int c = decltype(C)::v; ld16c(af[c], l16, rw, sAf + (unsigned)((c == 21 && kq >= 2) ? 0 : 4 * c) * 1024u); });
            }
        };
        auto expack = [&](const f32x16& s_, u32x4& p0, u32x4& p1) {
#pragma unroll
            for (int j = 0; j < 4; ++j) {
                p0[j] = cvtpk(__builtin_amdgcn_exp2f(s_[2 * j]), __builtin_amdgcn_exp2f(s_[2 * j + 1]));
                p1[j] = cvtpk(__builtin_amdgcn_exp2f(s_[8 + 2 * j]), __builtin_amdgcn_exp2f(s_[8 + 2 * j + 1]));
            }
        };
        f32x16 o;
        SB();
        {
            u32x4 p0, p1;
            expack(merge(S[0], S[1]), p0, p1);
            S[0] = score(IC<3>{}, negm); S[1] = score(IC<4>{}, negm);
            u32x4 d0, d1, e0, e1;
#pragma unroll
            for (int j = 0; j < 4; ++j) {
                const unsigned m0 = (mk[2 * j] & 0xFFFFu) | (mk[2 * j + 1] & 0xFFFF0000u), m1 = (mk[8 + 2 * j] & 0xFFFFu) | (mk[8 + 2 * j + 1] & 0xFFFF0000u);
                const unsigned a0 = p0[j], a1 = p1[j];
                d0[j] = a0 & m0; e0[j] = a0 & ~m0; d1[j] = a1 & m1; e1[j] = a1 & ~m1;
            }
            o = MFMA32(vf[0][0], __builtin_bit_cast(bf16x8, d0), zero);
            o = MFMA32(vf[0][1], __builtin_bit_cast(bf16x8, d1), o);
            rdv(IC<2>{}, vf[0]);
            o = MFMA32(vf[1][0], __builtin_bit_cast(bf16x8, e0), o);
            o = MFMA32(vf[1][1], __builtin_bit_cast(bf16x8, e1), o);
            slot(IC<0>{});
            SB();
        }
        sfor<2, 9>([&](auto P) {
            constexpr int pp = decltype(P)::v;
            if constexpr (pp + 1 < 9) rdv(IC<pp + 1>{}, vf[(pp + 1) & 1]);
            u32x4 p0, p1;
            expack(S[pp % 3], p0, p1);
            if constexpr (pp + 3 < 9) S[pp % 3] = score(IC<pp + 3>{}, negm);
            o = MFMA32(vf[pp & 1][0], __builtin_bit_cast(bf16x8, p0), o);
            o = MFMA32(vf[pp & 1][1], __builtin_bit_cast(bf16x8, p1), o);
            slot(IC<pp - 1>{});
            SB();
        });
        const float sc = g / o[8];
        st16c(nosent(pack8(o, 0, sc)), l16, rw, WS_A + (unsigned)((tb * NKS + NKS_H + hp * 2 + nh) * 1024));
    }
    STAMP(5);

    {
        const bool boss = blockIdx.x == 0 && w == 0;
        u32x4 ea[2];
        unsigned woff[2];
#pragma unroll
        for (int t = 0; t < 2; ++t) { const int row = 16 * kq + 4 * h + q; const int c16 = (t * 4 + blk * 2 + (p >> 1)) ^ (((row >> 1) & 1) << 2); woff[t] = (unsigned)(row * 128 + c16 * 16 + (p & 1) * 8); }
        f32x16 acc[2];
#pragma unroll
        for (int i = 0; i < 2; ++i)
#pragma unroll
            for (int j = 0; j < 16; ++j) acc[i][j] = 0.f;
        auto mm = [&](auto C) {
            constexpr int c = decltype(C)::v;
            if (c != 21 || kq < 2) {
                const LAS char* wb = L + fsl(c);
#pragma unroll
                for (int t = 0; t < 2; ++t) {
                    const bf16x8 wf = cat8(trd(wb + woff[t]), trd(wb + woff[t] + 8 * 128));
                    acc[t] = MFMA32(wf, __builtin_bit_cast(bf16x8, af[c]), acc[t]);
                }
            }
        };
        auto issueF1 = [&](auto C) { issueFp(decltype(C)::v, w, vW, vO); };
        waitv_bar<23>();
        STAMP(6);
        if (boss) { ld16c(ea[0], l16, rw, WS_FH); ld16c(ea[1], l16, rw, WS_FH + 1024u); }
        sfor<13, 19>(issueF1);
        sfor<0, 4>([&](auto C) { constexpr int c = decltype(C)::v; waitv_tie<28 - c>(af[c]); mm(C); });
        BAR_L(); sfor<19, 23>(issueF1);
        sfor<4, 8>([&](auto C) { constexpr int c = decltype(C)::v; waitv_tie<32 - c>(af[c]); mm(C); });
        BAR_L(); sfor<23, 27>(issueF1);
        sfor<8, 13>([&](auto C) { constexpr int c = decltype(C)::v; waitv_tie<36 - c>(af[c]); mm(C); });
        STAMP(7);
        waitv_bar<8>();
        sfor<13, 22>([&](auto C) { TIE(af[decltype(C)::v]); });
        sfor<27, 30>(issueF1);
        sfor<22, 30>([&](auto C) { constexpr int c = decltype(C)::v; ld16c(af[c], l16, rw, sAf + (unsigned)(NKS_H + 4 * (c - 22)) * 1024u); });
        bool epoch_done = !boss;
        if (boss) {
            TIE(ea[0]); TIE(ea[1]);
            bool ok = true;
#pragma unroll
            for (int j = 0; j < 2; ++j) { const unsigned a0 = ea[j][0], a1 = ea[j][1], a2 = ea[j][2], a3 = ea[j][3]; ok = ok && a0 == tok[0] && a1 == tok[1] && a2 == tok[0] && a3 == tok[1]; }
            if (__all(ok)) { epoch_done = true; if (lane == 0) { const unsigned long long e1 = epoch + 1; const u32x2 ev1 = {(unsigned)e1, (unsigned)(e1 >> 32)}; st8c(ev1, 0u, rw, WS_E); } }
        }
        sfor<13, 19>(mm);
        waitv_bar<15>();
        sfor<19, 22>(mm);
        STAMP(8);
        waitv<0>();
        sfor<22, 30>([&](auto C) { TIE(af[decltype(C)::v]); });
        {
            unsigned spins = 0;
            for (;;) {
                unsigned mxs = 0;
                sfor<22, 30>([&](auto C) { mxs = max(mxs, anysent(af[decltype(C)::v])); });
                if (__all(mxs != SENT) || ++spins >= SPIN_CAP) break;
                __builtin_amdgcn_s_sleep(1);
                sfor<22, 30>([&](auto C) { constexpr int c = decltype(C)::v; ld16c(af[c], l16, rw, sAf + (unsigned)(NKS_H + 4 * (c - 22)) * 1024u); });
                waitv<0>();
                sfor<22, 30>([&](auto C) { TIE(af[decltype(C)::v]); });
            }
        }
        STAMP(9);
        BAR_L();
        sfor<22, 30>(mm);
        BAR_L();
        STAMP(10);
        constexpr int RP = 272;
        {
            LAS char* dst = L + kq * (64 * RP) + (fm * 32 + r) * RP;
#pragma unroll
            for (int t = 0; t < 2; ++t)
#pragma unroll
                for (int g4 = 0; g4 < 4; ++g4)
                    *(LAS f32x4*)(dst + (t * 32 + g4 * 8 + 4 * h) * 4) = (f32x4){acc[t][4 * g4], acc[t][4 * g4 + 1], acc[t][4 * g4 + 2], acc[t][4 * g4 + 3]};
        }
        BAR_L();
        {
            const int tok_ = tid >> 3, oc = tid & 7;
            f32x4 a = {0.f, 0.f, 0.f, 0.f}, b = a;
#pragma unroll
            for (int k = 0; k < 4; ++k) {
                const LAS char* src = L + k * (64 * RP) + tok_ * RP + oc * 32;
                a += *(const LAS f32x4*)src; b += *(const LAS f32x4*)(src + 16);
            }
            u32x4 o = {cvtpk(a[0], a[1]), cvtpk(a[2], a[3]), cvtpk(b[0], b[1]), cvtpk(b[2], b[3])};
            *(GAS u32x4*)((char*)out + ((size_t)(rbF * 128 + rh * 64 + tok_) * 512 + ct * 64 + oc * 8) * 2) = o;
        }
        if (!epoch_done) {
            unsigned spins = 0;
            for (;;) {
                ld16c(ea[0], l16, rw, WS_FH); ld16c(ea[1], l16, rw, WS_FH + 1024u); waitv<0>(); TIE(ea[0]); TIE(ea[1]);
                bool ok = true;
#pragma unroll
                for (int j = 0; j < 2; ++j) { const unsigned a0 = ea[j][0], a1 = ea[j][1], a2 = ea[j][2], a3 = ea[j][3]; ok = ok && a0 == tok[0] && a1 == tok[1] && a2 == tok[0] && a3 == tok[1]; }
                if (__all(ok) || ++spins >= SPIN_CAP) break;
                __builtin_amdgcn_s_sleep(2);
            }
            if (lane == 0) { const unsigned long long e1 = epoch + 1; const u32x2 ev1 = {(unsigned)e1, (unsigned)(e1 >> 32)}; st8c(ev1, 0u, rw, WS_E); }
        }
    }
    asm volatile(".Lpf_end:");
}

extern "C" void kernel_launch(void* const* d_in, const int* in_sizes, int n_in, void* d_out, int out_size, void* d_ws, size_t ws_size, hipStream_t stream) {
    (void)hipFuncSetAttribute((const void*)k_fused, hipFuncAttributeMaxDynamicSharedMemorySize, LDS_BYTES);
    k_fused<<<256, 512, LDS_BYTES, stream>>>(d_in[0], d_in[1], d_in[2], d_in[4], d_in[5], d_in[6], (char*)d_ws, d_out);
}
```

```cpp
#include <hip/hip_runtime.h>
#include <hip/hip_bf16.h>
#include <stdint.h>

#define DI __device__ __forceinline__
#define LAS __attribute__((address_space(3)))
#define GAS __attribute__((address_space(1)))
typedef short bf16x8 __attribute__((ext_vector_type(8)));
typedef short s16x4 __attribute__((ext_vector_type(4)));
typedef float f32x16 __attribute__((ext_vector_type(16)));
typedef float f32x4 __attribute__((ext_vector_type(4)));
typedef float f32x2 __attribute__((ext_vector_type(2)));
typedef __bf16 bf16x2_t __attribute__((ext_vector_type(2)));
typedef unsigned u32x4 __attribute__((ext_vector_type(4)));
typedef unsigned u32x2 __attribute__((ext_vector_type(2)));
typedef __amdgpu_buffer_rsrc_t rsrc_t;

constexpr int S_ = 2048, D_ = 512, DFF = 1376;
constexpr int NKS_H = DFF / 16;
constexpr int NKS = NKS_H + 32;
constexpr unsigned WS_E = 0;
constexpr unsigned WS_FH = 8192;
constexpr unsigned WS_FK = 16384;
constexpr unsigned WS_FR = 32768;
constexpr unsigned WS_K = 1u << 20;
constexpr unsigned WS_V = WS_K + (2u << 20);
constexpr unsigned WS_A = WS_V + (2u << 20);
constexpr unsigned WS_END = WS_A + 64u * NKS * 1024u;
constexpr unsigned WS_DBG = WS_END;
constexpr unsigned SENT = 0xFFFFFFFFu;
constexpr unsigned SPIN_CAP = 1u << 18;

constexpr float QSCALE = 0.25f * 1.4426950408889634f;

#define MFMA32(a, b, c) __builtin_amdgcn_mfma_f32_32x32x16_bf16((a), (b), (c), 0, 0, 0)

DI unsigned cvtpk(float lo, float hi) { f32x2 v = {lo, hi}; bf16x2_t b = __builtin_convertvector(v, bf16x2_t); return __builtin_bit_cast(unsigned, b); }
DI u32x4 pack8(const f32x16& x, int s, float sc) {
    u32x4 p;
    p[0] = cvtpk(x[8 * s + 0] * sc, x[8 * s + 1] * sc); p[1] = cvtpk(x[8 * s + 2] * sc, x[8 * s + 3] * sc);
    p[2] = cvtpk(x[8 * s + 4] * sc, x[8 * s + 5] * sc); p[3] = cvtpk(x[8 * s + 6] * sc, x[8 * s + 7] * sc);
    return p;
}
DI u32x4 nosent(u32x4 p) {
#pragma unroll
    for (int j = 0; j < 4; ++j) { const unsigned v = p[j]; p[j] = v == SENT ? 0x7FC07FC0u : v; }
    return p;
}
DI void dma16(unsigned lds_dst, unsigned voff, rsrc_t rs, unsigned soff) {
    asm volatile("s_mov_b32 m0, %0\n\ts_nop 0\n\tbuffer_load_dwordx4 %1, %2, %3 offen lds" ::"s"(lds_dst), "v"(voff), "s"(rs), "s"(soff) : "memory");
}
DI void ld16c(u32x4& v, unsigned voff, rsrc_t rs, unsigned soff) {
    asm volatile("buffer_load_dwordx4 %0, %1, %2, %3 offen sc1" : "=&v"(v) : "v"(voff), "s"(rs), "s"(soff) : "memory");
}
DI void ld8c(u32x2& v, unsigned voff, rsrc_t rs, unsigned soff) {
    asm volatile("buffer_load_dwordx2 %0, %1, %2, %3 offen sc1" : "=&v"(v) : "v"(voff), "s"(rs), "s"(soff) : "memory");
}
DI void st16c(u32x4 v, unsigned voff, rsrc_t rs, unsigned soff) {
    asm volatile("buffer_store_dwordx4 %0, %1, %2, %3 offen sc1\n\ts_nop 1" ::"v"(v), "v"(voff), "s"(rs), "s"(soff) : "memory");
}
DI void st8c(u32x2 v, unsigned voff, rsrc_t rs, unsigned soff) {
    asm volatile("buffer_store_dwordx2 %0, %1, %2, %3 offen sc1" ::"v"(v), "v"(voff), "s"(rs), "s"(soff) : "memory");
}
template <int N> DI void waitv() { asm volatile("s_waitcnt vmcnt(%0)" ::"n"(N) : "memory"); }
template <int N> DI void waitv_bar() { asm volatile("s_waitcnt vmcnt(%0) lgkmcnt(0)\n\ts_barrier" ::"n"(N) : "memory"); }
DI void ld_epoch(unsigned long long& e, const char* ws) { asm volatile("s_load_dwordx2 %0, %1, 0x0" : "=s"(e) : "s"(ws) : "memory"); }
template <int N> DI void waitv_bar_e(unsigned long long& e) { asm volatile("s_waitcnt vmcnt(%1) lgkmcnt(0)\n\ts_barrier" : "+s"(e) : "n"(N) : "memory"); }
template <int N> DI void waitv_tie(u32x4& a) { asm volatile("s_waitcnt vmcnt(%1)" : "+v"(a) : "n"(N) : "memory"); }
template <int N> DI void waitv_tie2(u32x2& a) { asm volatile("s_waitcnt vmcnt(%1)" : "+v"(a) : "n"(N) : "memory"); }
DI void TIE(u32x4& x) { asm volatile("" : "+v"(x)); }
DI void TIE(u32x2& x) { asm volatile("" : "+v"(x)); }
#define BAR_L() asm volatile("s_waitcnt lgkmcnt(0)\n\ts_barrier" ::: "memory")
DI s16x4 trd(const LAS char* p) { return __builtin_bit_cast(s16x4, __builtin_amdgcn_ds_read_tr16_b64_v4i16((LAS s16x4*)p)); }
DI bf16x8 cat8(s16x4 lo, s16x4 hi) { return __builtin_shufflevector(lo, hi, 0, 1, 2, 3, 4, 5, 6, 7); }
DI rsrc_t mkrs(const void* p, unsigned bytes) { return __builtin_amdgcn_make_buffer_rsrc((void*)p, 0, (int)bytes, 0x00020000); }
DI unsigned umax3(unsigned a, unsigned b, unsigned c) { return max(max(a, b), c); }
DI unsigned anysent(const u32x4& a) { const unsigned a0 = a[0], a1 = a[1], a2 = a[2], a3 = a[3]; return max(max(a0, a1), max(a2, a3)); }

#define STAMP(i) do { } while (0)
#define STAMPC(i) do { } while (0)
#define STAMPW 0
#define FST(i) do { } while (0)

template <int I> struct IC { static constexpr int v = I; };
template <int B, int E, class F> DI void sfor(F&& f) { if constexpr (B < E) { f(IC<B>{}); sfor<B + 1, E>(f); } }

DI void block_map(int b, int& rb, int& hp) { const int xg = b & 7, idx = b >> 3; rb = (xg >> 2) * 8 + (idx >> 2); hp = (xg & 3) * 4 + (idx & 3); }

constexpr int KB = 1024;
__host__ __device__ constexpr int stS(int g) { return (g == 3 ? 0 : g) * 48 * KB; }
constexpr int L_W1 = 48 * KB;
constexpr int L_WQ = 0;
__host__ __device__ constexpr int fsl(int c) {
    constexpr int t[19] = {32, 40, 144, 48, 56, 64, 72, 128, 136, 0, 8, 16, 24, 80, 88, 96, 104, 112, 120};
    return t[c % 19] * KB;
}
constexpr int L_KST = 80 * KB, L_VST = 104 * KB;
constexpr int L_MISC = 152 * KB;
constexpr int L_GW = L_MISC, L_GP = L_MISC + 1024, L_QX = L_MISC + 2048, L_ONE = L_MISC + 6144, L_CNT = L_MISC + 6400, L_CNT2 = L_MISC + 6464, L_ZERO = L_MISC + 6656, L_DUM = L_MISC + 7680;
constexpr int LDS_BYTES = 163840;
#define KVFLAG 12
#define FHS 1
#define FHP 1
#define FHL 9
#define KVFI 16
#define KVM 44

#define PFC 6
#define PFCW 7
#define ROT (((idx & 3) + (idx >> 2)) & 3)
#define NPF 1
#define PFAW 4
#define PFB 2
#define PFWC 0
#define BAR_N(n) asm volatile("s_waitcnt lgkmcnt(" #n ")\n\ts_barrier" ::: "memory")
#define SB() __builtin_amdgcn_sched_barrier(0)
typedef short s16x2 __attribute__((ext_vector_type(2)));
DI unsigned relu2(unsigned v) { const s16x2 z = {0, 0}; return __builtin_bit_cast(unsigned, __builtin_elementwise_max(__builtin_bit_cast(s16x2, v), z)); }
DI const LAS char* lp(unsigned a) { return (const LAS char*)(uintptr_t)a; }
DI unsigned opq(unsigned v) { asm volatile("" : "+v"(v)); return v; }

__global__ __launch_bounds__(512, 2) void k_fused(const void* x, const void* wqkv, const void* wo, const void* w1, const void* w2, const void* gw, char* ws, void* out) {
    extern __shared__ __attribute__((aligned(16))) char smem[];
    LAS char* L = (LAS char*)smem;
    const int tid = threadIdx.x, lane = tid & 63;
    int w = __builtin_amdgcn_readfirstlane(tid >> 6);
    asm volatile("" : "+s"(w));
    const bool isC = w < 4;
    const int ww = w & 3, r = lane & 31, h = lane >> 5;
    int rb, hp; block_map(blockIdx.x, rb, hp);
    const int xg = blockIdx.x & 7, idx = blockIdx.x >> 3;
    const int rbF = 2 * xg + (idx >> 4), rh = (idx >> 3) & 1, ct = idx & 7;
    const unsigned ldsb = (unsigned)(uintptr_t)L;
    auto touch = [&](const char* a) { asm volatile("s_mov_b32 m0, %0\n\ts_nop 0\n\tglobal_load_lds_dword %1, off" ::"s"(ldsb + (unsigned)L_DUM), "v"(a) : "memory"); };
    asm volatile(".Lpf_start:");
    STAMP(0); STAMPC(12);
    if (w == PFCW) {
        unsigned long long pc0; unsigned clen, cback;
        asm volatile("s_getpc_b64 %0\n.Lpf_here:\n\ts_mov_b32 %1, .Lpf_end-.Lpf_start-4\n\ts_mov_b32 %2, .Lpf_here-.Lpf_start" : "=s"(pc0), "=s"(clen), "=s"(cback));
        const unsigned oc = min((unsigned)((idx * PFC + (lane < PFC ? lane : 0)) * 128), clen) & ~3u;
        const char* a = (const char*)(pc0 - cback) + oc;
        touch(a);
    }
    unsigned long long epoch;
    const rsrc_t rx = mkrs(x, S_ * D_ * 2), rq = mkrs(wqkv, D_ * 3 * D_ * 2), r1 = mkrs(w1, D_ * DFF * 2), rg = mkrs(gw, D_ * 2);
    const rsrc_t r2 = mkrs(w2, DFF * D_ * 2), ro = mkrs(wo, D_ * D_ * 2), rw = mkrs(ws, WS_END);
    const unsigned l16 = (unsigned)lane * 16u;
    const int tb = rb * 4 + ww;
    const int q = (lane & 15) >> 2, p = lane & 3, blk = (lane >> 4) & 1;
    const int u0 = (hp * 3 < 40) ? hp * 3 : 40;
    const int row8 = lane >> 3, pos = lane & 7;
    const u32x4 sentv = {SENT, SENT, SENT, SENT};

    const unsigned voX = (unsigned)((8 * ww + row8) * 1024 + (pos ^ (((8 * ww + row8) >> 1) & 7)) * 16);
    const unsigned sX = (unsigned)rb * 128u * 1024u;
    const int rot = ROT;
    auto issueX = [&](int g) {
#pragma unroll
        for (int t = 0; t < 8; ++t)
            dma16(ldsb + (unsigned)(g * 48 * KB + (t >> 2) * 16384 + (t & 3) * 4096) + (unsigned)ww * 1024u, voX, rx, sX + (unsigned)(((g + rot) & 3) * 256 + (t & 3) * 32768 + (t >> 2) * 128));
    };
    auto issueX3 = [&]() {
#pragma unroll
        for (int t = 0; t < 8; ++t)
            dma16(ldsb + (unsigned)((t >> 2) * 16384 + (t & 3) * 4096) + (unsigned)ww * 1024u, voX, rx, sX + (unsigned)(((3 + rot) & 3) * 256 + (t & 3) * 32768 + (t >> 2) * 128));
    };
    const unsigned voKV = (unsigned)((16 * ww + row8) * 3072 + 1024 + (hp & 1) * 1024 + (hp >> 1) * 128 + (pos ^ (((row8 >> 1) & 1) << 2)) * 16);
    auto issueKV = [&](int j) {
#pragma unroll
        for (int t = 0; t < 2; ++t) dma16(ldsb + (unsigned)(((j >> 1) == 3 ? 0 : (j >> 1)) * 48 * KB + 32 * KB + (j & 1) * 8192 + t * 1024) + (unsigned)ww * 2048u, voKV, rq, (unsigned)(((j + 2 * rot) & 7) * 64 + 8 * t) * 3072u);
    };
    auto mkvoW = [&](int ln) { const int r8 = ln >> 3, ps = ln & 7; return (unsigned)(r8 * 1024 + ct * 128 + (ps ^ (((r8 >> 1) & 1) << 2)) * 16); };
    auto mkvoO = [&](int ln) { const int r8 = ln >> 3, ps = ln & 7; return (unsigned)((8 * (r8 >> 2) + (r8 & 3)) * 1024 + ct * 128 + (ps ^ (((r8 >> 1) & 1) << 2)) * 16); };
    auto issueFp = [&](int c, int pi, unsigned vW, unsigned vO) {
        const unsigned dst = ldsb + (unsigned)fsl(c) + (unsigned)pi * 1024u;
        if (c < 22) dma16(dst, vW, r2, (unsigned)(c * 65536) + (unsigned)pi * 8192u);
        else dma16(dst, vO, ro, (unsigned)((c - 22) * 65536) + (unsigned)(16 * (pi >> 1) + 4 * (pi & 1)) * 1024u);
    };

    unsigned long long tok64 = 0;
    u32x2 tok;
    bf16x8 qf;
    auto mktok = [&]() {
        tok64 = epoch * 0x9E3779B97F4A7C15ull + 0x632BE59BD9B4E019ull;
        tok = (u32x2){(unsigned)tok64, (unsigned)(tok64 >> 32)};
    };

    const int hc = isC ? 0 : 1;
    auto issueHalf = [&](int g) {
        issueKV(2 * g + hc);
#pragma unroll
        for (int t = 0; t < 4; ++t)
            dma16(ldsb + (unsigned)(g * 48 * KB + t * 4096) + (unsigned)(hc * 16384 + ww * 1024), voX, rx, sX + (unsigned)(((g + rot) & 3) * 256 + t * 32768) + (unsigned)hc * 128u);
    };
    auto pfAddrA = [&]() -> const char* {
        const int g4 = xg & 3, r0 = 8 * ((xg >> 2) * 32 + idx) + 2 * ww, l = lane < 18 ? lane : 0;
        const int rr = l >= 7 ? 1 : 0, c = l - 7 * rr;
        const unsigned o1 = min((unsigned)((((r0 + rr) * 2752 + g4 * 768) & ~127) + c * 128), (unsigned)(D_ * DFF * 2 - 4));
        const unsigned oq = (unsigned)((r0 + ((l >> 1) & 1)) * 3072 + g4 * 256 + (l & 1) * 128);
        return l < 14 ? (const char*)w1 + o1 : (const char*)wqkv + oq;
    };
    auto pfAddrB = [&]() -> const char* {
        const int l = (lane & 31) < 15 ? (lane & 31) : 0;
        return l < 4 ? (const char*)wo + (unsigned)((blockIdx.x * 16 + ww * 4 + l) * 128) : (const char*)w2 + (unsigned)(min((int)(blockIdx.x * 43 + ww * 11 + (l - 4)), (int)(blockIdx.x * 43 + 42)) * 128);
    };
    auto pfA = [&]() { touch((PFB == 0 && lane >= 32) ? pfAddrB() : pfAddrA()); };
    auto pfB = [&]() { touch(pfAddrB()); };
    if (w == 0) dma16(ldsb + L_GW, (l16 + (unsigned)rot * 256u) & 1023u, rg, 0u);
    issueHalf(0);
    FST(26);
    if (PFAW < 4 ? isC : !isC) pfA();
    FST(27);
    issueHalf(1);
    FST(15);
    if (isC) {
        const unsigned sKV = ((hp & 1) ? WS_V : WS_K) + (unsigned)(((hp & ~1) * 64 + tb) * 2048);
        unsigned long long ep; ld_epoch(ep, ws);
        const int xrow = ww * 32 + r;
        unsigned xoff[4];
#pragma unroll
        for (int s = 0; s < 4; ++s) { const int c = 2 * s + h; xoff[s] = ldsb + (unsigned)(xrow * 128 + ((c ^ ((xrow >> 1) & 7)) * 16)); }
        const unsigned kvoff = (unsigned)((8 * h + q) * 128 + ((blk * 2 + (p >> 1)) ^ (((q >> 1) & 1) << 2)) * 16 + (p & 1) * 8);
        const unsigned gwoff = ldsb + (((lane & 15) == blk) ? (unsigned)(L_GW + h * 16) : (unsigned)L_ZERO);
        bf16x8 xf[32];
        f32x16 acc[2];
#pragma unroll
        for (int j = 0; j < 16; ++j) { acc[0][j] = 0.f; acc[1][j] = 0.f; }
        f32x4 ag = {0.f, 0.f, 0.f, 0.f};
        FST(13);
        waitv_bar_e<(PFAW < 4 ? 6 + NPF : 6)>(ep);
        STAMP(1);
        epoch = ep; mktok();
        sfor<0, 4>([&](auto G) {
            constexpr int g = decltype(G)::v;
            const unsigned hi = stS(g) >= 65536 ? 65536u : 0u;
#pragma unroll
            for (int c = 0; c < 2; ++c)
#pragma unroll
                for (int s = 0; s < 4; ++s) xf[g * 8 + c * 4 + s] = *(const LAS bf16x8*)(lp((stS(g) >= 65536 ? opq(xoff[s] + hi) : xoff[s])) + (stS(g) - (int)hi + c * 16384));
            const unsigned kb0 = opq(ldsb + kvoff + (unsigned)(stS(g) + 32 * KB)), kb1 = opq(ldsb + (kvoff ^ 64u) + (unsigned)(stS(g) + 32 * KB));
            constexpr int DK = 4;
            bf16x8 wr[DK + 1], gf[4];
            sfor<0, 16 + DK>([&](auto I) {
                constexpr int i = decltype(I)::v;
                if constexpr (i < 16) {
                    const LAS char* pw = lp((i & 1) ? kb1 : kb0) + (i >> 3) * 8192 + ((i >> 1) & 3) * 2048;
                    wr[i % (DK + 1)] = cat8(trd(pw), trd(pw + 512));
                    if constexpr ((i & 1) == 0) gf[(i >> 1) & 3] = *(const LAS bf16x8*)(lp(gwoff) + (g * 128 + (i >> 1) * 16) * 2);
                }
                if constexpr (i >= DK) {
                    constexpr int j = i - DK;
                    acc[j & 1] = MFMA32(wr[j % (DK + 1)], xf[g * 8 + (j >> 1)], acc[j & 1]);
                    if constexpr ((j & 1) == 1) ag = __builtin_amdgcn_mfma_f32_16x16x32_bf16(gf[(j >> 1) & 3], xf[g * 8 + (j >> 1)], ag, 0, 0, 0);
                }
                SB();
            });
            FST(g);
            if constexpr (g == 0) waitv_bar<0>(); else if constexpr (g < 3) BAR_L();
        });
        STAMP(2);
        if (lane < 16) {
            ((LAS float*)(L + L_GP))[ww * 32 + lane] = 1.f / (1.f + __expf(-ag[0]));
            ((LAS float*)(L + L_GP))[ww * 32 + 16 + lane] = 1.f / (1.f + __expf(-ag[1]));
        }
        const float og = 1.f - ((const LAS float*)(L + L_GP))[xrow];
        const unsigned ql = (hp & 1) ? (unsigned)(2 * r + h) * 16u : l16;
        const unsigned w1off = ldsb + (unsigned)((8 * h + q) * 192 + blk * 32 + p * 8);
        const unsigned w1a = opq(w1off + (unsigned)L_W1), w1b = opq(w1off + (unsigned)(L_W1 + 60 * KB));
        const unsigned wqo = opq(ldsb + (unsigned)(L_WQ + (8 * h + q) * 64 + blk * 32 + p * 8));
        const unsigned sA = WS_A + (unsigned)(tb * NKS + 2 * u0) * 1024u;
        const int dup = hp * 3 - u0;
#pragma unroll
        for (int n = 0; n < 4; ++n) st16c(pack8(acc[n >> 1], n & 1, 1.f), ql, rw, sKV + (unsigned)((n >> 1) * 131072 + (n & 1) * 1024));
        BAR_L();
        FST(4);
        {
            constexpr int DP = 6, RP_ = 8;
            bf16x8 wr[RP_];
            f32x16 ah[2];
            u32x4 pk;
            unsigned wql = 0;
            sfor<0, 128 + DP>([&](auto I) {
                constexpr int i = decltype(I)::v;
                if constexpr (i == KVFI) {
                    FST(20);
                    waitv<0>();
                    FST(21);
                    if (lane == 0) st8c(tok, 0u, rw, WS_FK + (unsigned)(((hp >> 1) * 64 + tb) * 2 + (hp & 1)) * 8u);
                }
                if constexpr (i == 16) { FST(5); BAR_N(12); FST(6); }
                if constexpr (i == KVM || i == 96) {
                    asm volatile("" ::: "memory");
                    if (lane == 0) __hip_atomic_fetch_add((LAS unsigned*)(L + L_CNT2 + (i == KVM ? 0 : 4)), 1u, __ATOMIC_RELAXED, __HIP_MEMORY_SCOPE_WORKGROUP);
                    asm volatile("" ::: "memory");
                    FST(i == KVM ? 7 : 8);
                }
                if constexpr (i == 90) wql = __hip_atomic_load((LAS unsigned*)(L + L_CNT2 + 12), __ATOMIC_RELAXED, __HIP_MEMORY_SCOPE_WORKGROUP);
                if constexpr (i == 96) {
                    if (__builtin_expect(__builtin_amdgcn_readfirstlane(wql) < 4u, 0)) {
                        unsigned spins = 0;
                        do { __builtin_amdgcn_s_sleep(1); wql = __hip_atomic_load((LAS unsigned*)(L + L_CNT2 + 12), __ATOMIC_RELAXED, __HIP_MEMORY_SCOPE_WORKGROUP); } while (__builtin_amdgcn_readfirstlane(wql) < 4u && ++spins < SPIN_CAP);
                    }
                    asm volatile("" ::: "memory");
                    FST(23);
                }
                if constexpr (i < 96) {
                    constexpr int t = i / 32, c = (i % 32) / 4, s = i % 4;
                    const LAS char* pw = lp(c < 5 ? w1a : w1b) + ((c < 5 ? c : c - 5) * 12 * KB + s * 16 * 192 + t * 64);
                    wr[i % RP_] = cat8(trd(pw), trd(pw + 4 * 192));
                } else if constexpr (i < 128) {
                    const LAS char* pw = lp(wqo) + (i - 96) * 1024;
                    wr[i % RP_] = cat8(trd(pw), trd(pw + 256));
                }
                if constexpr (i >= DP) {
                    constexpr int j = i - DP, T = j / 32, jj = j % 32;
                    if constexpr (jj == 0) {
                        f32x16 z;
#pragma unroll
                        for (int e = 0; e < 16; ++e) z[e] = 0.f;
                        ah[T & 1] = MFMA32(wr[j % RP_], xf[jj], z);
                    } else ah[T & 1] = MFMA32(wr[j % RP_], xf[jj], ah[T & 1]);
                    if constexpr (T >= 1 && jj >= 1 && jj < 9) {
                        constexpr int n = (jj - 1) / 4, qd = (jj - 1) % 4, t = T - 1;
                        pk[qd] = relu2(cvtpk(ah[t & 1][8 * n + 2 * qd] * og, ah[t & 1][8 * n + 2 * qd + 1] * og));
                        if constexpr (qd == 3) { if (t == 0 || t >= dup) st16c(pk, l16, rw, sA + (unsigned)((2 * t + n) * 1024)); }
                    }
                }
                SB();
            });
            qf = __builtin_bit_cast(bf16x8, pack8(ah[1], 0, QSCALE));
            *(LAS u32x4*)(L + L_QX + ww * 1024 + l16) = pack8(ah[1], 1, QSCALE);
        }
        STAMP(3);
        BAR_L();
    } else {
        unsigned long long ep; ld_epoch(ep, ws);
        if (w == 5 && lane == 0) { *(LAS unsigned*)(L + L_CNT) = 0u; *(LAS unsigned*)(L + L_CNT2) = 0u; *(LAS unsigned*)(L + L_CNT2 + 4) = 0u; *(LAS unsigned*)(L + L_CNT2 + 8) = 0u; *(LAS unsigned*)(L + L_CNT2 + 12) = 0u; }
        if (w == 6) *(LAS u32x4*)(L + L_ZERO + l16) = (u32x4){0u, 0u, 0u, 0u};
        unsigned voW1[3];
#pragma unroll
        for (int t = 0; t < 3; ++t) { const int li = 64 * t + lane, row = li / 12, c = li - row * 12; voW1[t] = (unsigned)((16 * ww + row) * 2752 + u0 * 64 + c * 16); }
        const unsigned voQ = (unsigned)((32 * ww + (lane >> 2)) * 3072 + hp * 64 + (lane & 3) * 16);
        auto issueW1 = [&](int c) {
#pragma unroll
            for (int t = 0; t < 3; ++t) dma16(ldsb + (unsigned)(L_W1 + c * 12 * KB + t * 1024) + (unsigned)ww * 3072u, voW1[t], r1, (unsigned)((c + 2 * rot) & 7) * 64u * 2752u);
        };
        auto issueWQ = [&](int pc) {
#pragma unroll
            for (int t = 0; t < 2; ++t) dma16(ldsb + (unsigned)(L_WQ + pc * 8192 + t * 1024) + (unsigned)ww * 2048u, voQ, rq, (unsigned)(((pc + rot) & 3) * 128 + 16 * t) * 3072u);
        };
        const unsigned vW = mkvoW(lane);
        auto issueF2 = [&](int c) { issueFp(c, 2 * ww, vW, 0u); issueFp(c, 2 * ww + 1, vW, 0u); };
        FST(13);
        waitv_bar_e<(PFAW >= 4 ? 6 + NPF : 6)>(ep);
        STAMP(1);
        epoch = ep; mktok();
        issueX(2); issueKV(4); issueKV(5);
        if (PFB == 1) pfB();
        waitv_bar<(PFB == 1 ? 12 + NPF : 12)>();
        FST(0);
        issueX3(); issueKV(6); issueKV(7);
        if (PFB == 2) pfB();
        waitv_bar<(PFB == 2 ? 12 + NPF : 12)>();
        FST(1);
        issueW1(0); issueW1(1); issueW1(2); issueW1(3);
        if (PFB == 3) pfB();
        waitv_bar<(PFB == 3 ? 12 + NPF : 12)>();
        FST(2);
        issueW1(4); issueW1(5); issueW1(6); issueW1(7);
        STAMP(2);
        waitv_bar<12>();
        FST(4);
        if (w == 4) *(LAS u32x4*)(L + L_GW + l16) = (u32x4){0x3f803f80u, 0x3f803f80u, 0x3f803f80u, 0x3f803f80u};
        issueWQ(0); issueWQ(1); issueWQ(2); issueWQ(3);
        {
            const unsigned sO = WS_A + (unsigned)((tb * NKS + NKS_H + hp * 2) * 1024);
            st16c(sentv, l16, rw, sO); st16c(sentv, l16, rw, sO + 1024u);
        }
        FST(5);
        waitv_bar<10>();
        FST(6);
        auto waitC = [&](int n) {
            unsigned spins = 0;
            while (__hip_atomic_load((LAS unsigned*)(L + L_CNT2 + 4 * n), __ATOMIC_RELAXED, __HIP_MEMORY_SCOPE_WORKGROUP) < 4u && ++spins < SPIN_CAP) __builtin_amdgcn_s_sleep(1);
            asm volatile("" ::: "memory");
        };
        waitv<0>();
        if (lane == 0) __hip_atomic_fetch_add((LAS unsigned*)(L + L_CNT2 + 12), 1u, __ATOMIC_RELAXED, __HIP_MEMORY_SCOPE_WORKGROUP);
        FST(22);
        {
            unsigned spins = 0;
            if (w == 4) {
                unsigned voFK;
                { const int l = lane & 31, i = l < 24 ? l >> 1 : 0, kt = rb * 4 - 8 + i;
                  voFK = WS_FK + (unsigned)(((hp >> 1) * 64 + (kt < 0 ? 0 : kt)) * 2 + (l & 1)) * 8u; }
                u32x2 fkv;
                waitC(0);
                FST(20);
                do { ld8c(fkv, voFK, rw, 0u); waitv_tie2<0>(fkv); } while (!__all(fkv[0] == tok[0] && fkv[1] == tok[1]) && ++spins < SPIN_CAP);
                if (lane == 0) __hip_atomic_store((LAS unsigned*)(L + L_CNT2 + 8), 1u, __ATOMIC_RELAXED, __HIP_MEMORY_SCOPE_WORKGROUP);
            } else {
                while (__hip_atomic_load((LAS unsigned*)(L + L_CNT2 + 8), __ATOMIC_RELAXED, __HIP_MEMORY_SCOPE_WORKGROUP) == 0u && ++spins < SPIN_CAP) __builtin_amdgcn_s_sleep(1);
            }
            asm volatile("" ::: "memory");
        }
        FST(24);
        {
            u32x4 kst[6], vst[6];
            const int hh = ww & 1;
#pragma unroll
            for (int t = 0; t < 6; ++t) {
                const int i = (ww >> 1) + 2 * t, kt = rb * 4 - 8 + i;
                const unsigned sst = (unsigned)(((hp * 64 + (kt < 0 ? 0 : kt)) * 2 + hh) * 1024);
                ld16c(kst[t], kt < 0 ? (unsigned)(lane & 32) * 16u : l16, rw, WS_K + sst); ld16c(vst[t], l16, rw, WS_V + sst);
            }
            FST(7);
            waitC(1);
            FST(21);
            sfor<0, 6>([&](auto T) {
                constexpr int t = decltype(T)::v;
                const int i = (ww >> 1) + 2 * t, kt = rb * 4 - 8 + i;
                waitv_tie<11 - 2 * t>(kst[t]);
                *(LAS u32x4*)(L + L_KST + (hh * 12 + i) * 1024 + l16) = kst[t];
                waitv_tie<10 - 2 * t>(vst[t]);
                *(LAS u32x4*)(L + L_VST + (hh * 12 + i) * 1024 + l16) = kt < 0 ? (u32x4){0u, 0u, 0u, 0u} : vst[t];
            });
        }
        STAMP(3);
        BAR_L();
    }
    STAMP(4);

    int lane2 = lane;
    asm volatile("" : "+v"(lane2));
    const unsigned vW = mkvoW(lane2), vO = mkvoO(lane2);
    const int fm = w & 1, kq = w >> 1;
    const unsigned sAf = WS_A + (unsigned)((rbF * 4 + rh * 2 + fm) * NKS + kq) * 1024u;
    u32x4 af[30];
    {
        const int nh = w >> 2, m = ww;
        const LAS char* kb = L + L_KST + (nh * 12 + m) * 1024 + l16;
        bf16x8 kf[9];
#pragma unroll
        for (int i = 0; i < 9; ++i) kf[i] = *(const LAS bf16x8*)(kb + ((i + 8) % 9) * 1024);
        if (!isC) qf = *(const LAS bf16x8*)(L + L_QX + m * 1024 + l16);
        const float g = ((const LAS float*)(L + L_GP))[m * 32 + r];
        const unsigned voFH = WS_FR + (unsigned)((rbF * 16 + rh * 8 + ct) * 16 + (lane2 & 15)) * 8u;
        u32x2 fhv;
        auto raise = [&]() {
            if (isC) {
                waitv<0>();
                unsigned cnt = 0;
                if (lane == 0) cnt = __hip_atomic_fetch_add((LAS unsigned*)(L + L_CNT), 1u, __ATOMIC_RELAXED, __HIP_MEMORY_SCOPE_WORKGROUP);
                cnt = __builtin_amdgcn_readfirstlane(cnt);
                if (cnt == 3u && lane2 < 17) st8c(tok, lane2 < 16 ? WS_FR + (unsigned)((rb * 16 + lane2) * 16 + hp) * 8u : WS_FH + (unsigned)(rb * 16 + hp) * 8u, rw, 0u);
            }
        };
        if constexpr (FHP == 0) raise();
        const int t0 = tb >= 8 ? 0 : 8 - tb;
        const unsigned vstep = blk ? 0u : 1024u;
        const unsigned vb0 = ldsb + (blk ? (unsigned)(L_GW + (1 - h) * 128) : (unsigned)(L_VST + (nh * 12 + m) * 1024 + (4 * h + q) * 32 + p * 8));
        unsigned mk[16];
#pragma unroll
        for (int j = 0; j < 16; ++j) mk[j] = ((j & 3) + 8 * (j >> 2) + 4 * h <= r) ? 0xFFFFFFFFu : 0u;
        f32x16 zero;
#pragma unroll
        for (int j = 0; j < 16; ++j) zero[j] = 0.f;
        const float NEG = -INFINITY;
        auto score = [&](auto P, const f32x16& c) -> f32x16 { return MFMA32(kf[decltype(P)::v], qf, c); };
        auto merge = [&](const f32x16& a, const f32x16& b) {
            f32x16 o_;
#pragma unroll
            for (int j = 0; j < 16; ++j) { const float x_ = a[j], y_ = b[j]; o_[j] = __builtin_bit_cast(float, (__builtin_bit_cast(unsigned, x_) & mk[j]) | (__builtin_bit_cast(unsigned, y_) & ~mk[j])); }
            return o_;
        };
        float mx = NEG;
        auto red = [&](const f32x16& s_) {
#pragma unroll
            for (int j = 0; j < 16; j += 2) mx = fmaxf(fmaxf(s_[j], s_[j + 1]), mx);
        };
        {
            f32x16 S[4];
            S[0] = score(IC<0>{}, zero); S[1] = score(IC<1>{}, zero); S[2] = score(IC<2>{}, zero); S[3] = score(IC<3>{}, zero);
            SB();
            if constexpr (FHP == 1) { raise(); SB(); }
            red(merge(S[0], S[1]));
            S[0] = score(IC<4>{}, zero); S[1] = score(IC<5>{}, zero);
            SB();
            sfor<2, 9>([&](auto P) {
                constexpr int pp = decltype(P)::v;
                if constexpr (FHP == pp) { raise(); SB(); }
                if constexpr (FHL == pp) ld8c(fhv, voFH, rw, 0u);
                if (isC) { issueFp(pp - 2, 2 * ww, vW, vO); issueFp(pp - 2, 2 * ww + 1, vW, vO); }
                red(S[pp % 4]);
                if constexpr (pp + 4 < 9) S[pp % 4] = score(IC<pp + 4>{}, zero);
                SB();
            });
        }
        {
            const u32x2 sw = __builtin_amdgcn_permlane32_swap(__builtin_bit_cast(unsigned, mx), __builtin_bit_cast(unsigned, mx), false, false);
            const unsigned s0_ = sw[0], s1_ = sw[1];
            mx = fmaxf(__builtin_bit_cast(float, s0_), __builtin_bit_cast(float, s1_));
        }
        f32x16 negm;
#pragma unroll
        for (int j = 0; j < 16; ++j) negm[j] = -mx;
        FST(9);
        f32x16 S[3];
        S[0] = score(IC<0>{}, negm); S[1] = score(IC<1>{}, negm); S[2] = score(IC<2>{}, negm);
        bf16x8 vf[2][2];
        auto rdv = [&](auto P, bf16x8 (&v)[2]) {
            constexpr int pp = decltype(P)::v, tile = (pp + 8) % 9;
            const unsigned dz = tile < t0 ? (unsigned)(L_ZERO - L_GW) : 0u;
            const LAS char* p0 = lp(vb0 + vstep * (unsigned)tile + (blk ? dz : 0u));
            v[0] = cat8(trd(p0), trd(p0 + 256)); v[1] = cat8(trd(p0 + 512), trd(p0 + 768));
        };
        rdv(IC<0>{}, vf[0]); rdv(IC<1>{}, vf[1]);
        if constexpr (FHP == 9) raise();
        if constexpr (FHL == 9) ld8c(fhv, voFH, rw, 0u);
        FST(10);
        auto slot = [&](auto SL) {
            constexpr int sl = decltype(SL)::v;
            if constexpr (sl < 2) sfor<7 + 3 * sl, 10 + 3 * sl>([&](auto C) { issueFp(decltype(C)::v, w, vW, vO); });
            if constexpr (sl == FHL - 10) ld8c(fhv, voFH, rw, 0u);
            if constexpr (sl == FHS) {
                FST(11);
                waitv_tie2<(FHL >= 10 ? 3 * ((FHS < 1 ? FHS : 1) - (FHL - 10) > 0 ? (FHS < 1 ? FHS : 1) - (FHL - 10) : 0) : 3 * (FHS < 2 ? FHS + 1 : 2))>(fhv);
                unsigned spins = 0;
                while (!__all(fhv[0] == tok[0] && fhv[1] == tok[1]) && ++spins < SPIN_CAP) { __builtin_amdgcn_s_sleep(1); ld8c(fhv, voFH, rw, 0u); waitv_tie2<0>(fhv); }
                FST(12);
            }
            if constexpr (sl >= FHS) {
                constexpr int ns = 8 - FHS, c0 = 22 * (sl - FHS) / ns, c1 = 22 * (sl - FHS + 1) / ns;
                sfor<c0, c1>([&](auto C) { constexpr int c = decltype(C)::v; ld16c(af[c], l16, rw, sAf + (unsigned)((c == 21 && kq >= 2) ? 0 : 4 * c) * 1024u); });
            }
        };
        auto expack = [&](const f32x16& s_, u32x4& p0, u32x4& p1) {
#pragma unroll
            for (int j = 0; j < 4; ++j) {
                p0[j] = cvtpk(__builtin_amdgcn_exp2f(s_[2 * j]), __builtin_amdgcn_exp2f(s_[2 * j + 1]));
                p1[j] = cvtpk(__builtin_amdgcn_exp2f(s_[8 + 2 * j]), __builtin_amdgcn_exp2f(s_[8 + 2 * j + 1]));
            }
        };
        f32x16 o;
        SB();
        {
            u32x4 p0, p1;
            expack(merge(S[0], S[1]), p0, p1);
            S[0] = score(IC<3>{}, negm); S[1] = score(IC<4>{}, negm);
            u32x4 d0, d1, e0, e1;
#pragma unroll
            for (int j = 0; j < 4; ++j) {
                const unsigned m0 = (mk[2 * j] & 0xFFFFu) | (mk[2 * j + 1] & 0xFFFF0000u), m1 = (mk[8 + 2 * j] & 0xFFFFu) | (mk[8 + 2 * j + 1] & 0xFFFF0000u);
                const unsigned a0 = p0[j], a1 = p1[j];
                d0[j] = a0 & m0; e0[j] = a0 & ~m0; d1[j] = a1 & m1; e1[j] = a1 & ~m1;
            }
            o = MFMA32(vf[0][0], __builtin_bit_cast(bf16x8, d0), zero);
            o = MFMA32(vf[0][1], __builtin_bit_cast(bf16x8, d1), o);
            rdv(IC<2>{}, vf[0]);
            o = MFMA32(vf[1][0], __builtin_bit_cast(bf16x8, e0), o);
            o = MFMA32(vf[1][1], __builtin_bit_cast(bf16x8, e1), o);
            slot(IC<0>{});
            SB();
        }
        sfor<2, 9>([&](auto P) {
            constexpr int pp = decltype(P)::v;
            if constexpr (pp + 1 < 9) rdv(IC<pp + 1>{}, vf[(pp + 1) & 1]);
            u32x4 p0, p1;
            expack(S[pp % 3], p0, p1);
            if constexpr (pp + 3 < 9) S[pp % 3] = score(IC<pp + 3>{}, negm);
            o = MFMA32(vf[pp & 1][0], __builtin_bit_cast(bf16x8, p0), o);
            o = MFMA32(vf[pp & 1][1], __builtin_bit_cast(bf16x8, p1), o);
            slot(IC<pp - 1>{});
            SB();
        });
        const float sc = g / o[8];
        st16c(nosent(pack8(o, 0, sc)), l16, rw, WS_A + (unsigned)((tb * NKS + NKS_H + hp * 2 + nh) * 1024));
    }
    STAMP(5);

    {
        const bool boss = blockIdx.x == 0 && w == 0;
        u32x4 ea[2];
        unsigned woff[2];
#pragma unroll
        for (int t = 0; t < 2; ++t) { const int row = 16 * kq + 4 * h + q; const int c16 = (t * 4 + blk * 2 + (p >> 1)) ^ (((row >> 1) & 1) << 2); woff[t] = (unsigned)(row * 128 + c16 * 16 + (p & 1) * 8); }
        f32x16 acc[2];
#pragma unroll
        for (int i = 0; i < 2; ++i)
#pragma unroll
            for (int j = 0; j < 16; ++j) acc[i][j] = 0.f;
        auto mm = [&](auto C) {
            constexpr int c = decltype(C)::v;
            if (c != 21 || kq < 2) {
                const LAS char* wb = L + fsl(c);
#pragma unroll
                for (int t = 0; t < 2; ++t) {
                    const bf16x8 wf = cat8(trd(wb + woff[t]), trd(wb + woff[t] + 8 * 128));
                    acc[t] = MFMA32(wf, __builtin_bit_cast(bf16x8, af[c]), acc[t]);
                }
            }
        };
        auto issueF1 = [&](auto C) { issueFp(decltype(C)::v, w, vW, vO); };
        waitv_bar<(FHS == 0 ? 21 : 23)>();
        STAMP(6);
        if (boss) { ld16c(ea[0], l16, rw, WS_FH); ld16c(ea[1], l16, rw, WS_FH + 1024u); }
        sfor<13, 19>(issueF1);
        sfor<0, 4>([&](auto C) { constexpr int c = decltype(C)::v; waitv_tie<28 - c>(af[c]); mm(C); });
        BAR_L(); sfor<19, 23>(issueF1);
        sfor<4, 8>([&](auto C) { constexpr int c = decltype(C)::v; waitv_tie<32 - c>(af[c]); mm(C); });
        BAR_L(); sfor<23, 27>(issueF1);
        sfor<8, 13>([&](auto C) { constexpr int c = decltype(C)::v; waitv_tie<36 - c>(af[c]); mm(C); });
        STAMP(7);
        waitv_bar<8>();
        sfor<13, 22>([&](auto C) { TIE(af[decltype(C)::v]); });
        sfor<27, 30>(issueF1);
        sfor<22, 30>([&](auto C) { constexpr int c = decltype(C)::v; ld16c(af[c], l16, rw, sAf + (unsigned)(NKS_H + 4 * (c - 22)) * 1024u); });
        bool epoch_done = !boss;
        if (boss) {
            TIE(ea[0]); TIE(ea[1]);
            bool ok = true;
#pragma unroll
            for (int j = 0; j < 2; ++j) { const unsigned a0 = ea[j][0], a1 = ea[j][1], a2 = ea[j][2], a3 = ea[j][3]; ok = ok && a0 == tok[0] && a1 == tok[1] && a2 == tok[0] && a3 == tok[1]; }
            if (__all(ok)) { epoch_done = true; if (lane == 0) { const unsigned long long e1 = epoch + 1; const u32x2 ev1 = {(unsigned)e1, (unsigned)(e1 >> 32)}; st8c(ev1, 0u, rw, WS_E); } }
        }
        sfor<13, 19>(mm);
        waitv_bar<15>();
        sfor<19, 22>(mm);
        STAMP(8);
        waitv<0>();
        sfor<22, 30>([&](auto C) { TIE(af[decltype(C)::v]); });
        {
            unsigned spins = 0;
            for (;;) {
                unsigned mxs = 0;
                sfor<22, 30>([&](auto C) { mxs = max(mxs, anysent(af[decltype(C)::v])); });
                if (__all(mxs != SENT) || ++spins >= SPIN_CAP) break;
                __builtin_amdgcn_s_sleep(1);
                sfor<22, 30>([&](auto C) { constexpr int c = decltype(C)::v; ld16c(af[c], l16, rw, sAf + (unsigned)(NKS_H + 4 * (c - 22)) * 1024u); });
                waitv<0>();
                sfor<22, 30>([&](auto C) { TIE(af[decltype(C)::v]); });
            }
        }
        STAMP(9);
        BAR_L();
        sfor<22, 30>(mm);
        BAR_L();
        STAMP(10);
        constexpr int RP = 272;
        {
            LAS char* dst = L + kq * (64 * RP) + (fm * 32 + r) * RP;
#pragma unroll
            for (int t = 0; t < 2; ++t)
#pragma unroll
                for (int g4 = 0; g4 < 4; ++g4)
                    *(LAS f32x4*)(dst + (t * 32 + g4 * 8 + 4 * h) * 4) = (f32x4){acc[t][4 * g4], acc[t][4 * g4 + 1], acc[t][4 * g4 + 2], acc[t][4 * g4 + 3]};
        }
        BAR_L();
        {
            const int tok_ = tid >> 3, oc = tid & 7;
            f32x4 a = {0.f, 0.f, 0.f, 0.f}, b = a;
#pragma unroll
            for (int k = 0; k < 4; ++k) {
                const LAS char* src = L + k * (64 * RP) + tok_ * RP + oc * 32;
                a += *(const LAS f32x4*)src; b += *(const LAS f32x4*)(src + 16);
            }
            u32x4 o = {cvtpk(a[0], a[1]), cvtpk(a[2], a[3]), cvtpk(b[0], b[1]), cvtpk(b[2], b[3])};
            *(GAS u32x4*)((char*)out + ((size_t)(rbF * 128 + rh * 64 + tok_) * 512 + ct * 64 + oc * 8) * 2) = o;
        }
        if (!epoch_done) {
            unsigned spins = 0;
            for (;;) {
                ld16c(ea[0], l16, rw, WS_FH); ld16c(ea[1], l16, rw, WS_FH + 1024u); waitv<0>(); TIE(ea[0]); TIE(ea[1]);
                bool ok = true;
#pragma unroll
                for (int j = 0; j < 2; ++j) { const unsigned a0 = ea[j][0], a1 = ea[j][1], a2 = ea[j][2], a3 = ea[j][3]; ok = ok && a0 == tok[0] && a1 == tok[1] && a2 == tok[0] && a3 == tok[1]; }
                if (__all(ok) || ++spins >= SPIN_CAP) break;
                __builtin_amdgcn_s_sleep(2);
            }
            if (lane == 0) { const unsigned long long e1 = epoch + 1; const u32x2 ev1 = {(unsigned)e1, (unsigned)(e1 >> 32)}; st8c(ev1, 0u, rw, WS_E); }
        }
    }
    asm volatile(".Lpf_end:");
}

extern "C" void kernel_launch(void* const* d_in, const int* in_sizes, int n_in, void* d_out, int out_size, void* d_ws, size_t ws_size, hipStream_t stream) {
    (void)hipFuncSetAttribute((const void*)k_fused, hipFuncAttributeMaxDynamicSharedMemorySize, LDS_BYTES);
    k_fused<<<256, 512, LDS_BYTES, stream>>>(d_in[0], d_in[1], d_in[2], d_in[4], d_in[5], d_in[6], (char*)d_ws, d_out);
}
```

```cpp
#include <hip/hip_runtime.h>
#include <hip/hip_bf16.h>
#include <stdint.h>

#define DI __device__ __forceinline__
#define LAS __attribute__((address_space(3)))
#define GAS __attribute__((address_space(1)))
typedef short bf16x8 __attribute__((ext_vector_type(8)));
typedef short s16x4 __attribute__((ext_vector_type(4)));
typedef float f32x16 __attribute__((ext_vector_type(16)));
typedef float f32x4 __attribute__((ext_vector_type(4)));
typedef float f32x2 __attribute__((ext_vector_type(2)));
typedef __bf16 bf16x2_t __attribute__((ext_vector_type(2)));
typedef unsigned u32x4 __attribute__((ext_vector_type(4)));
typedef unsigned u32x2 __attribute__((ext_vector_type(2)));
typedef __amdgpu_buffer_rsrc_t rsrc_t;

constexpr int S_ = 2048, D_ = 512, DFF = 1376;
constexpr int NKS_H = DFF / 16;
constexpr int NKS = NKS_H + 32;
constexpr unsigned WS_E = 0;
constexpr unsigned WS_FH = 8192;
constexpr unsigned WS_FK = 16384;
constexpr unsigned WS_FR = 32768;
constexpr unsigned WS_K = 1u << 20;
constexpr unsigned WS_V = WS_K + (2u << 20);
constexpr unsigned WS_A = WS_V + (2u << 20);
constexpr unsigned WS_END = WS_A + 64u * NKS * 1024u;
constexpr unsigned WS_DBG = WS_END;
constexpr unsigned SENT = 0xFFFFFFFFu;
constexpr unsigned SPIN_CAP = 1u << 18;

constexpr float QSCALE = 0.25f * 1.4426950408889634f;

#define MFMA32(a, b, c) __builtin_amdgcn_mfma_f32_32x32x16_bf16((a), (b), (c), 0, 0, 0)

DI unsigned cvtpk(float lo, float hi) { f32x2 v = {lo, hi}; bf16x2_t b = __builtin_convertvector(v, bf16x2_t); return __builtin_bit_cast(unsigned, b); }
DI u32x4 pack8(const f32x16& x, int s, float sc) {
    u32x4 p;
    p[0] = cvtpk(x[8 * s + 0] * sc, x[8 * s + 1] * sc); p[1] = cvtpk(x[8 * s + 2] * sc, x[8 * s + 3] * sc);
    p[2] = cvtpk(x[8 * s + 4] * sc, x[8 * s + 5] * sc); p[3] = cvtpk(x[8 * s + 6] * sc, x[8 * s + 7] * sc);
    return p;
}
DI u32x4 nosent(u32x4 p) {
#pragma unroll
    for (int j = 0; j < 4; ++j) { const unsigned v = p[j]; p[j] = v == SENT ? 0x7FC07FC0u : v; }
    return p;
}
DI void dma16(unsigned lds_dst, unsigned voff, rsrc_t rs, unsigned soff) {
    asm volatile("s_mov_b32 m0, %0\n\ts_nop 0\n\tbuffer_load_dwordx4 %1, %2, %3 offen lds" ::"s"(lds_dst), "v"(voff), "s"(rs), "s"(soff) : "memory");
}
DI void ld16c(u32x4& v, unsigned voff, rsrc_t rs, unsigned soff) {
    asm volatile("buffer_load_dwordx4 %0, %1, %2, %3 offen sc1" : "=&v"(v) : "v"(voff), "s"(rs), "s"(soff) : "memory");
}
DI void ld8c(u32x2& v, unsigned voff, rsrc_t rs, unsigned soff) {
    asm volatile("buffer_load_dwordx2 %0, %1, %2, %3 offen sc1" : "=&v"(v) : "v"(voff), "s"(rs), "s"(soff) : "memory");
}
DI void st16c(u32x4 v, unsigned voff, rsrc_t rs, unsigned soff) {
    asm volatile("buffer_store_dwordx4 %0, %1, %2, %3 offen sc1\n\ts_nop 1" ::"v"(v), "v"(voff), "s"(rs), "s"(soff) : "memory");
}
DI void st8c(u32x2 v, unsigned voff, rsrc_t rs, unsigned soff) {
    asm volatile("buffer_store_dwordx2 %0, %1, %2, %3 offen sc1" ::"v"(v), "v"(voff), "s"(rs), "s"(soff) : "memory");
}
template <int N> DI void waitv() { asm volatile("s_waitcnt vmcnt(%0)" ::"n"(N) : "memory"); }
template <int N> DI void waitv_bar() { asm volatile("s_waitcnt vmcnt(%0) lgkmcnt(0)\n\ts_barrier" ::"n"(N) : "memory"); }
DI void ld_epoch(unsigned long long& e, const char* ws) { asm volatile("s_load_dwordx2 %0, %1, 0x0" : "=s"(e) : "s"(ws) : "memory"); }
template <int N> DI void waitv_bar_e(unsigned long long& e) { asm volatile("s_waitcnt vmcnt(%1) lgkmcnt(0)\n\ts_barrier" : "+s"(e) : "n"(N) : "memory"); }
template <int N> DI void waitv_tie(u32x4& a) { asm volatile("s_waitcnt vmcnt(%1)" : "+v"(a) : "n"(N) : "memory"); }
template <int N> DI void waitv_tie2(u32x2& a) { asm volatile("s_waitcnt vmcnt(%1)" : "+v"(a) : "n"(N) : "memory"); }
DI void TIE(u32x4& x) { asm volatile("" : "+v"(x)); }
DI void TIE(u32x2& x) { asm volatile("" : "+v"(x)); }
#define BAR_L() asm volatile("s_waitcnt lgkmcnt(0)\n\ts_barrier" ::: "memory")
DI s16x4 trd(const LAS char* p) { return __builtin_bit_cast(s16x4, __builtin_amdgcn_ds_read_tr16_b64_v4i16((LAS s16x4*)p)); }
DI bf16x8 cat8(s16x4 lo, s16x4 hi) { return __builtin_shufflevector(lo, hi, 0, 1, 2, 3, 4, 5, 6, 7); }
DI rsrc_t mkrs(const void* p, unsigned bytes) { return __builtin_amdgcn_make_buffer_rsrc((void*)p, 0, (int)bytes, 0x00020000); }
DI unsigned umax3(unsigned a, unsigned b, unsigned c) { return max(max(a, b), c); }
DI unsigned anysent(const u32x4& a) { const unsigned a0 = a[0], a1 = a[1], a2 = a[2], a3 = a[3]; return max(max(a0, a1), max(a2, a3)); }

#define STAMP(i) do { } while (0)
#define STAMPC(i) do { } while (0)
#define STAMPW 0
#define FST(i) do { } while (0)

template <int I> struct IC { static constexpr int v = I; };
template <int B, int E, class F> DI void sfor(F&& f) { if constexpr (B < E) { f(IC<B>{}); sfor<B + 1, E>(f); } }

DI void block_map(int b, int& rb, int& hp) { const int xg = b & 7, idx = b >> 3; rb = (xg >> 2) * 8 + (idx >> 2); hp = (xg & 3) * 4 + (idx & 3); }

constexpr int KB = 1024;
__host__ __device__ constexpr int stS(int g) { return (g == 3 ? 0 : g) * 48 * KB; }
constexpr int L_W1 = 48 * KB;
constexpr int L_WQ = 0;
__host__ __device__ constexpr int fsl(int c) {
    constexpr int t[19] = {32, 40, 144, 48, 56, 64, 72, 128, 136, 0, 8, 16, 24, 80, 88, 96, 104, 112, 120};
    return t[c % 19] * KB;
}
constexpr int L_KST = 80 * KB, L_VST = 104 * KB;
constexpr int L_MISC = 152 * KB;
constexpr int L_GW = L_MISC, L_GP = L_MISC + 1024, L_QX = L_MISC + 2048, L_ONE = L_MISC + 6144, L_CNT = L_MISC + 6400, L_CNT2 = L_MISC + 6464, L_ZERO = L_MISC + 6656, L_DUM = L_MISC + 7680;
constexpr int LDS_BYTES = 163840;
#define KVFLAG 12
#define FHS 2
#define FHP 1
#define FHL 10
#define KVFI 16
#define OUTM 1
#define FROT 0
#define KVM 44

#define PFC 6
#define PFCW 7
#define ROT (((idx & 3) + (idx >> 2)) & 3)
#define NPF 1
#define PFAW 4
#define PFB 2
#define PFWC 0
#define BAR_N(n) asm volatile("s_waitcnt lgkmcnt(" #n ")\n\ts_barrier" ::: "memory")
#define SB() __builtin_amdgcn_sched_barrier(0)
typedef short s16x2 __attribute__((ext_vector_type(2)));
DI unsigned relu2(unsigned v) { const s16x2 z = {0, 0}; return __builtin_bit_cast(unsigned, __builtin_elementwise_max(__builtin_bit_cast(s16x2, v), z)); }
DI const LAS char* lp(unsigned a) { return (const LAS char*)(uintptr_t)a; }
DI unsigned opq(unsigned v) { asm volatile("" : "+v"(v)); return v; }

__global__ __launch_bounds__(512, 2) void k_fused(const void* x, const void* wqkv, const void* wo, const void* w1, const void* w2, const void* gw, char* ws, void* out) {
    extern __shared__ __attribute__((aligned(16))) char smem[];
    LAS char* L = (LAS char*)smem;
    const int tid = threadIdx.x, lane = tid & 63;
    int w = __builtin_amdgcn_readfirstlane(tid >> 6);
    asm volatile("" : "+s"(w));
    const bool isC = w < 4;
    const int ww = w & 3, r = lane & 31, h = lane >> 5;
    int rb, hp; block_map(blockIdx.x, rb, hp);
    const int xg = blockIdx.x & 7, idx = blockIdx.x >> 3;
    const int rbF = 2 * xg + (idx >> 4), rh = (idx >> 3) & 1, ct = idx & 7;
    const unsigned ldsb = (unsigned)(uintptr_t)L;
    auto touch = [&](const char* a) { asm volatile("s_mov_b32 m0, %0\n\ts_nop 0\n\tglobal_load_lds_dword %1, off" ::"s"(ldsb + (unsigned)L_DUM), "v"(a) : "memory"); };
    asm volatile(".Lpf_start:");
    STAMP(0); STAMPC(12);
    if (w == PFCW) {
        unsigned long long pc0; unsigned clen, cback;
        asm volatile("s_getpc_b64 %0\n.Lpf_here:\n\ts_mov_b32 %1, .Lpf_end-.Lpf_start-4\n\ts_mov_b32 %2, .Lpf_here-.Lpf_start" : "=s"(pc0), "=s"(clen), "=s"(cback));
        const unsigned oc = min((unsigned)((idx * PFC + (lane < PFC ? lane : 0)) * 128), clen) & ~3u;
        const char* a = (const char*)(pc0 - cback) + oc;
        touch(a);
    }
    unsigned long long epoch;
    const rsrc_t rx = mkrs(x, S_ * D_ * 2), rq = mkrs(wqkv, D_ * 3 * D_ * 2), r1 = mkrs(w1, D_ * DFF * 2), rg = mkrs(gw, D_ * 2);
    const rsrc_t r2 = mkrs(w2, DFF * D_ * 2), ro = mkrs(wo, D_ * D_ * 2), rw = mkrs(ws, WS_END);
    const unsigned l16 = (unsigned)lane * 16u;
    const int tb = rb * 4 + ww;
    const int q = (lane & 15) >> 2, p = lane & 3, blk = (lane >> 4) & 1;
    const int u0 = (hp * 3 < 40) ? hp * 3 : 40;
    const int row8 = lane >> 3, pos = lane & 7;
    const u32x4 sentv = {SENT, SENT, SENT, SENT};

    const unsigned voX = (unsigned)((8 * ww + row8) * 1024 + (pos ^ (((8 * ww + row8) >> 1) & 7)) * 16);
    const unsigned sX = (unsigned)rb * 128u * 1024u;
    const int rot = ROT;
    auto issueX = [&](int g) {
#pragma unroll
        for (int t = 0; t < 8; ++t)
            dma16(ldsb + (unsigned)(g * 48 * KB + (t >> 2) * 16384 + (t & 3) * 4096) + (unsigned)ww * 1024u, voX, rx, sX + (unsigned)(((g + rot) & 3) * 256 + (t & 3) * 32768 + (t >> 2) * 128));
    };
    auto issueX3 = [&]() {
#pragma unroll
        for (int t = 0; t < 8; ++t)
            dma16(ldsb + (unsigned)((t >> 2) * 16384 + (t & 3) * 4096) + (unsigned)ww * 1024u, voX, rx, sX + (unsigned)(((3 + rot) & 3) * 256 + (t & 3) * 32768 + (t >> 2) * 128));
    };
    const unsigned voKV = (unsigned)((16 * ww + row8) * 3072 + 1024 + (hp & 1) * 1024 + (hp >> 1) * 128 + (pos ^ (((row8 >> 1) & 1) << 2)) * 16);
    auto issueKV = [&](int j) {
#pragma unroll
        for (int t = 0; t < 2; ++t) dma16(ldsb + (unsigned)(((j >> 1) == 3 ? 0 : (j >> 1)) * 48 * KB + 32 * KB + (j & 1) * 8192 + t * 1024) + (unsigned)ww * 2048u, voKV, rq, (unsigned)(((j + 2 * rot) & 7) * 64 + 8 * t) * 3072u);
    };
    auto mkvoW = [&](int ln) { const int r8 = ln >> 3, ps = ln & 7; return (unsigned)(r8 * 1024 + ct * 128 + (ps ^ (((r8 >> 1) & 1) << 2)) * 16); };
    auto mkvoO = [&](int ln) { const int r8 = ln >> 3, ps = ln & 7; return (unsigned)((8 * (r8 >> 2) + (r8 & 3)) * 1024 + ct * 128 + (ps ^ (((r8 >> 1) & 1) << 2)) * 16); };
    const int rotF = FROT;
    auto ccF = [&](int c) { const int t = c + rotF; return t >= 22 ? t - 22 : t; };
    auto issueFp = [&](int c, int pi, unsigned vW, unsigned vO) {
        const unsigned dst = ldsb + (unsigned)fsl(c) + (unsigned)pi * 1024u;
        if (c < 22) dma16(dst, vW, r2, (unsigned)(ccF(c) * 65536) + (unsigned)pi * 8192u);
        else dma16(dst, vO, ro, (unsigned)((c - 22) * 65536) + (unsigned)(16 * (pi >> 1) + 4 * (pi & 1)) * 1024u);
    };

    unsigned long long tok64 = 0;
    u32x2 tok;
    bf16x8 qf;
    auto mktok = [&]() {
        tok64 = epoch * 0x9E3779B97F4A7C15ull + 0x632BE59BD9B4E019ull;
        tok = (u32x2){(unsigned)tok64, (unsigned)(tok64 >> 32)};
    };

    const int hc = isC ? 0 : 1;
    auto issueHalf = [&](int g) {
        issueKV(2 * g + hc);
#pragma unroll
        for (int t = 0; t < 4; ++t)
            dma16(ldsb + (unsigned)(g * 48 * KB + t * 4096) + (unsigned)(hc * 16384 + ww * 1024), voX, rx, sX + (unsigned)(((g + rot) & 3) * 256 + t * 32768) + (unsigned)hc * 128u);
    };
    auto pfAddrA = [&]() -> const char* {
        const int g4 = xg & 3, r0 = 8 * ((xg >> 2) * 32 + idx) + 2 * ww, l = lane < 18 ? lane : 0;
        const int rr = l >= 7 ? 1 : 0, c = l - 7 * rr;
        const unsigned o1 = min((unsigned)((((r0 + rr) * 2752 + g4 * 768) & ~127) + c * 128), (unsigned)(D_ * DFF * 2 - 4));
        const unsigned oq = (unsigned)((r0 + ((l >> 1) & 1)) * 3072 + g4 * 256 + (l & 1) * 128);
        return l < 14 ? (const char*)w1 + o1 : (const char*)wqkv + oq;
    };
    auto pfAddrB = [&]() -> const char* {
        const int l = (lane & 31) < 15 ? (lane & 31) : 0;
        return l < 4 ? (const char*)wo + (unsigned)((blockIdx.x * 16 + ww * 4 + l) * 128) : (const char*)w2 + (unsigned)(min((int)(blockIdx.x * 43 + ww * 11 + (l - 4)), (int)(blockIdx.x * 43 + 42)) * 128);
    };
    auto pfA = [&]() { touch((PFB == 0 && lane >= 32) ? pfAddrB() : pfAddrA()); };
    auto pfB = [&]() { touch(pfAddrB()); };
    if (w == 0) dma16(ldsb + L_GW, (l16 + (unsigned)rot * 256u) & 1023u, rg, 0u);
    issueHalf(0);
    FST(26);
    if (PFAW < 4 ? isC : !isC) pfA();
    FST(27);
    issueHalf(1);
    FST(15);
    if (isC) {
        const unsigned sKV = ((hp & 1) ? WS_V : WS_K) + (unsigned)(((hp & ~1) * 64 + tb) * 2048);
        unsigned long long ep; ld_epoch(ep, ws);
        const int xrow = ww * 32 + r;
        unsigned xoff[4];
#pragma unroll
        for (int s = 0; s < 4; ++s) { const int c = 2 * s + h; xoff[s] = ldsb + (unsigned)(xrow * 128 + ((c ^ ((xrow >> 1) & 7)) * 16)); }
        const unsigned kvoff = (unsigned)((8 * h + q) * 128 + ((blk * 2 + (p >> 1)) ^ (((q >> 1) & 1) << 2)) * 16 + (p & 1) * 8);
        const unsigned gwoff = ldsb + (((lane & 15) == blk) ? (unsigned)(L_GW + h * 16) : (unsigned)L_ZERO);
        bf16x8 xf[32];
        f32x16 acc[2];
#pragma unroll
        for (int j = 0; j < 16; ++j) { acc[0][j] = 0.f; acc[1][j] = 0.f; }
        f32x4 ag = {0.f, 0.f, 0.f, 0.f};
        FST(13);
        waitv_bar_e<(PFAW < 4 ? 6 + NPF : 6)>(ep);
        STAMP(1);
        epoch = ep; mktok();
        sfor<0, 4>([&](auto G) {
            constexpr int g = decltype(G)::v;
            const unsigned hi = stS(g) >= 65536 ? 65536u : 0u;
#pragma unroll
            for (int c = 0; c < 2; ++c)
#pragma unroll
                for (int s = 0; s < 4; ++s) xf[g * 8 + c * 4 + s] = *(const LAS bf16x8*)(lp((stS(g) >= 65536 ? opq(xoff[s] + hi) : xoff[s])) + (stS(g) - (int)hi + c * 16384));
            const unsigned kb0 = opq(ldsb + kvoff + (unsigned)(stS(g) + 32 * KB)), kb1 = opq(ldsb + (kvoff ^ 64u) + (unsigned)(stS(g) + 32 * KB));
            constexpr int DK = 4;
            bf16x8 wr[DK + 1], gf[4];
            sfor<0, 16 + DK>([&](auto I) {
                constexpr int i = decltype(I)::v;
                if constexpr (i < 16) {
                    const LAS char* pw = lp((i & 1) ? kb1 : kb0) + (i >> 3) * 8192 + ((i >> 1) & 3) * 2048;
                    wr[i % (DK + 1)] = cat8(trd(pw), trd(pw + 512));
                    if constexpr ((i & 1) == 0) gf[(i >> 1) & 3] = *(const LAS bf16x8*)(lp(gwoff) + (g * 128 + (i >> 1) * 16) * 2);
                }
                if constexpr (i >= DK) {
                    constexpr int j = i - DK;
                    acc[j & 1] = MFMA32(wr[j % (DK + 1)], xf[g * 8 + (j >> 1)], acc[j & 1]);
                    if constexpr ((j & 1) == 1) ag = __builtin_amdgcn_mfma_f32_16x16x32_bf16(gf[(j >> 1) & 3], xf[g * 8 + (j >> 1)], ag, 0, 0, 0);
                }
                SB();
            });
            FST(g);
            if constexpr (g == 0) waitv_bar<0>(); else if constexpr (g < 3) BAR_L();
        });
        STAMP(2);
        if (lane < 16) {
            ((LAS float*)(L + L_GP))[ww * 32 + lane] = 1.f / (1.f + __expf(-ag[0]));
            ((LAS float*)(L + L_GP))[ww * 32 + 16 + lane] = 1.f / (1.f + __expf(-ag[1]));
        }
        const float og = 1.f - ((const LAS float*)(L + L_GP))[xrow];
        const unsigned ql = (hp & 1) ? (unsigned)(2 * r + h) * 16u : l16;
        const unsigned w1off = ldsb + (unsigned)((8 * h + q) * 192 + blk * 32 + p * 8);
        const unsigned w1a = opq(w1off + (unsigned)L_W1), w1b = opq(w1off + (unsigned)(L_W1 + 60 * KB));
        const unsigned wqo = opq(ldsb + (unsigned)(L_WQ + (8 * h + q) * 64 + blk * 32 + p * 8));
        const unsigned sA = WS_A + (unsigned)(tb * NKS + 2 * u0) * 1024u;
        const int dup = hp * 3 - u0;
#pragma unroll
        for (int n = 0; n < 4; ++n) st16c(pack8(acc[n >> 1], n & 1, 1.f), ql, rw, sKV + (unsigned)((n >> 1) * 131072 + (n & 1) * 1024));
        BAR_L();
        FST(4);
        {
            constexpr int DP = 6, RP_ = 8;
            bf16x8 wr[RP_];
            f32x16 ah[2];
            u32x4 pk;
            unsigned wql = 0;
            sfor<0, 128 + DP>([&](auto I) {
                constexpr int i = decltype(I)::v;
                if constexpr (i == KVFI) {
                    FST(20);
                    waitv<0>();
                    FST(21);
                    if (lane == 0) st8c(tok, 0u, rw, WS_FK + (unsigned)(((hp >> 1) * 64 + tb) * 2 + (hp & 1)) * 8u);
                }
                if constexpr (i == 16) { FST(5); BAR_N(12); FST(6); }
                if constexpr (i == KVM || i == 96) {
                    asm volatile("" ::: "memory");
                    if (lane == 0) __hip_atomic_fetch_add((LAS unsigned*)(L + L_CNT2 + (i == KVM ? 0 : 4)), 1u, __ATOMIC_RELAXED, __HIP_MEMORY_SCOPE_WORKGROUP);
                    asm volatile("" ::: "memory");
                    FST(i == KVM ? 7 : 8);
                }
                if constexpr (i == 90) wql = __hip_atomic_load((LAS unsigned*)(L + L_CNT2 + 12), __ATOMIC_RELAXED, __HIP_MEMORY_SCOPE_WORKGROUP);
                if constexpr (i == 96) {
                    if (__builtin_expect(__builtin_amdgcn_readfirstlane(wql) < 4u, 0)) {
                        unsigned spins = 0;
                        do { __builtin_amdgcn_s_sleep(1); wql = __hip_atomic_load((LAS unsigned*)(L + L_CNT2 + 12), __ATOMIC_RELAXED, __HIP_MEMORY_SCOPE_WORKGROUP); } while (__builtin_amdgcn_readfirstlane(wql) < 4u && ++spins < SPIN_CAP);
                    }
                    asm volatile("" ::: "memory");
                    FST(23);
                }
                if constexpr (i < 96) {
                    constexpr int t = i / 32, c = (i % 32) / 4, s = i % 4;
                    const LAS char* pw = lp(c < 5 ? w1a : w1b) + ((c < 5 ? c : c - 5) * 12 * KB + s * 16 * 192 + t * 64);
                    wr[i % RP_] = cat8(trd(pw), trd(pw + 4 * 192));
                } else if constexpr (i < 128) {
                    const LAS char* pw = lp(wqo) + (i - 96) * 1024;
                    wr[i % RP_] = cat8(trd(pw), trd(pw + 256));
                }
                if constexpr (i >= DP) {
                    constexpr int j = i - DP, T = j / 32, jj = j % 32;
                    if constexpr (jj == 0) {
                        f32x16 z;
#pragma unroll
                        for (int e = 0; e < 16; ++e) z[e] = 0.f;
                        ah[T & 1] = MFMA32(wr[j % RP_], xf[jj], z);
                    } else ah[T & 1] = MFMA32(wr[j % RP_], xf[jj], ah[T & 1]);
                    if constexpr (T >= 1 && jj >= 1 && jj < 9) {
                        constexpr int n = (jj - 1) / 4, qd = (jj - 1) % 4, t = T - 1;
                        pk[qd] = relu2(cvtpk(ah[t & 1][8 * n + 2 * qd] * og, ah[t & 1][8 * n + 2 * qd + 1] * og));
                        if constexpr (qd == 3) { if (t == 0 || t >= dup) st16c(pk, l16, rw, sA + (unsigned)((2 * t + n) * 1024)); }
                    }
                }
                SB();
            });
            qf = __builtin_bit_cast(bf16x8, pack8(ah[1], 0, QSCALE));
            *(LAS u32x4*)(L + L_QX + ww * 1024 + l16) = pack8(ah[1], 1, QSCALE);
        }
        STAMP(3);
        BAR_L();
    } else {
        unsigned long long ep; ld_epoch(ep, ws);
        if (w == 5 && lane == 0) { *(LAS unsigned*)(L + L_CNT) = 0u; *(LAS unsigned*)(L + L_CNT2) = 0u; *(LAS unsigned*)(L + L_CNT2 + 4) = 0u; *(LAS unsigned*)(L + L_CNT2 + 8) = 0u; *(LAS unsigned*)(L + L_CNT2 + 12) = 0u; }
        if (w == 6) *(LAS u32x4*)(L + L_ZERO + l16) = (u32x4){0u, 0u, 0u, 0u};
        unsigned voW1[3];
#pragma unroll
        for (int t = 0; t < 3; ++t) { const int li = 64 * t + lane, row = li / 12, c = li - row * 12; voW1[t] = (unsigned)((16 * ww + row) * 2752 + u0 * 64 + c * 16); }
        const unsigned voQ = (unsigned)((32 * ww + (lane >> 2)) * 3072 + hp * 64 + (lane & 3) * 16);
        auto issueW1 = [&](int c) {
#pragma unroll
            for (int t = 0; t < 3; ++t) dma16(ldsb + (unsigned)(L_W1 + c * 12 * KB + t * 1024) + (unsigned)ww * 3072u, voW1[t], r1, (unsigned)((c + 2 * rot) & 7) * 64u * 2752u);
        };
        auto issueWQ = [&](int pc) {
#pragma unroll
            for (int t = 0; t < 2; ++t) dma16(ldsb + (unsigned)(L_WQ + pc * 8192 + t * 1024) + (unsigned)ww * 2048u, voQ, rq, (unsigned)(((pc + rot) & 3) * 128 + 16 * t) * 3072u);
        };
        const unsigned vW = mkvoW(lane);
        auto issueF2 = [&](int c) { issueFp(c, 2 * ww, vW, 0u); issueFp(c, 2 * ww + 1, vW, 0u); };
        FST(13);
        waitv_bar_e<(PFAW >= 4 ? 6 + NPF : 6)>(ep);
        STAMP(1);
        epoch = ep; mktok();
        issueX(2); issueKV(4); issueKV(5);
        if (PFB == 1) pfB();
        waitv_bar<(PFB == 1 ? 12 + NPF : 12)>();
        FST(0);
        issueX3(); issueKV(6); issueKV(7);
        if (PFB == 2) pfB();
        waitv_bar<(PFB == 2 ? 12 + NPF : 12)>();
        FST(1);
        issueW1(0); issueW1(1); issueW1(2); issueW1(3);
        if (PFB == 3) pfB();
        waitv_bar<(PFB == 3 ? 12 + NPF : 12)>();
        FST(2);
        issueW1(4); issueW1(5); issueW1(6); issueW1(7);
        STAMP(2);
        waitv_bar<12>();
        FST(4);
        if (w == 4) *(LAS u32x4*)(L + L_GW + l16) = (u32x4){0x3f803f80u, 0x3f803f80u, 0x3f803f80u, 0x3f803f80u};
        issueWQ(0); issueWQ(1); issueWQ(2); issueWQ(3);
        {
            const unsigned sO = WS_A + (unsigned)((tb * NKS + NKS_H + hp * 2) * 1024);
            st16c(sentv, l16, rw, sO); st16c(sentv, l16, rw, sO + 1024u);
        }
        FST(5);
        waitv_bar<10>();
        FST(6);
        auto waitC = [&](int n) {
            unsigned spins = 0;
            while (__hip_atomic_load((LAS unsigned*)(L + L_CNT2 + 4 * n), __ATOMIC_RELAXED, __HIP_MEMORY_SCOPE_WORKGROUP) < 4u && ++spins < SPIN_CAP) __builtin_amdgcn_s_sleep(1);
            asm volatile("" ::: "memory");
        };
        waitv<0>();
        if (lane == 0) __hip_atomic_fetch_add((LAS unsigned*)(L + L_CNT2 + 12), 1u, __ATOMIC_RELAXED, __HIP_MEMORY_SCOPE_WORKGROUP);
        FST(22);
        {
            unsigned spins = 0;
            if (w == 4) {
                unsigned voFK;
                { const int l = lane & 31, i = l < 24 ? l >> 1 : 0, kt = rb * 4 - 8 + i;
                  voFK = WS_FK + (unsigned)(((hp >> 1) * 64 + (kt < 0 ? 0 : kt)) * 2 + (l & 1)) * 8u; }
                u32x2 fkv;
                waitC(0);
                FST(20);
                do { ld8c(fkv, voFK, rw, 0u); waitv_tie2<0>(fkv); } while (!__all(fkv[0] == tok[0] && fkv[1] == tok[1]) && ++spins < SPIN_CAP);
                if (lane == 0) __hip_atomic_store((LAS unsigned*)(L + L_CNT2 + 8), 1u, __ATOMIC_RELAXED, __HIP_MEMORY_SCOPE_WORKGROUP);
            } else {
                while (__hip_atomic_load((LAS unsigned*)(L + L_CNT2 + 8), __ATOMIC_RELAXED, __HIP_MEMORY_SCOPE_WORKGROUP) == 0u && ++spins < SPIN_CAP) __builtin_amdgcn_s_sleep(1);
            }
            asm volatile("" ::: "memory");
        }
        FST(24);
        {
            u32x4 kst[6], vst[6];
            const int hh = ww & 1;
#pragma unroll
            for (int t = 0; t < 6; ++t) {
                const int i = (ww >> 1) + 2 * t, kt = rb * 4 - 8 + i;
                const unsigned sst = (unsigned)(((hp * 64 + (kt < 0 ? 0 : kt)) * 2 + hh) * 1024);
                ld16c(kst[t], kt < 0 ? (unsigned)(lane & 32) * 16u : l16, rw, WS_K + sst); ld16c(vst[t], l16, rw, WS_V + sst);
            }
            FST(7);
            waitC(1);
            FST(21);
            sfor<0, 6>([&](auto T) {
                constexpr int t = decltype(T)::v;
                const int i = (ww >> 1) + 2 * t, kt = rb * 4 - 8 + i;
                waitv_tie<11 - 2 * t>(kst[t]);
                *(LAS u32x4*)(L + L_KST + (hh * 12 + i) * 1024 + l16) = kst[t];
                waitv_tie<10 - 2 * t>(vst[t]);
                *(LAS u32x4*)(L + L_VST + (hh * 12 + i) * 1024 + l16) = kt < 0 ? (u32x4){0u, 0u, 0u, 0u} : vst[t];
            });
        }
        STAMP(3);
        BAR_L();
    }
    STAMP(4);

    int lane2 = lane;
    asm volatile("" : "+v"(lane2));
    const unsigned vW = mkvoW(lane2), vO = mkvoO(lane2);
    const int fm = w & 1, kq = w >> 1;
    const unsigned sAf = WS_A + (unsigned)((rbF * 4 + rh * 2 + fm) * NKS + kq) * 1024u;
    u32x4 af[30];
    {
        const int nh = w >> 2, m = ww;
        const LAS char* kb = L + L_KST + (nh * 12 + m) * 1024 + l16;
        bf16x8 kf[9];
#pragma unroll
        for (int i = 0; i < 9; ++i) kf[i] = *(const LAS bf16x8*)(kb + ((i + 8) % 9) * 1024);
        if (!isC) qf = *(const LAS bf16x8*)(L + L_QX + m * 1024 + l16);
        const float g = ((const LAS float*)(L + L_GP))[m * 32 + r];
        const unsigned voFH = WS_FR + (unsigned)((rbF * 16 + rh * 8 + ct) * 16 + (lane2 & 15)) * 8u;
        u32x2 fhv;
        auto raise = [&]() {
            if (isC) {
                waitv<0>();
                unsigned cnt = 0;
                if (lane == 0) cnt = __hip_atomic_fetch_add((LAS unsigned*)(L + L_CNT), 1u, __ATOMIC_RELAXED, __HIP_MEMORY_SCOPE_WORKGROUP);
                cnt = __builtin_amdgcn_readfirstlane(cnt);
                if (cnt == 3u && lane2 < 17) st8c(tok, lane2 < 16 ? WS_FR + (unsigned)((rb * 16 + lane2) * 16 + hp) * 8u : WS_FH + (unsigned)(rb * 16 + hp) * 8u, rw, 0u);
            }
        };
        if constexpr (FHP == 0) raise();
        const int t0 = tb >= 8 ? 0 : 8 - tb;
        const unsigned vstep = blk ? 0u : 1024u;
        const unsigned vb0 = ldsb + (blk ? (unsigned)(L_GW + (1 - h) * 128) : (unsigned)(L_VST + (nh * 12 + m) * 1024 + (4 * h + q) * 32 + p * 8));
        unsigned mk[16];
#pragma unroll
        for (int j = 0; j < 16; ++j) mk[j] = ((j & 3) + 8 * (j >> 2) + 4 * h <= r) ? 0xFFFFFFFFu : 0u;
        f32x16 zero;
#pragma unroll
        for (int j = 0; j < 16; ++j) zero[j] = 0.f;
        const float NEG = -INFINITY;
        auto score = [&](auto P, const f32x16& c) -> f32x16 { return MFMA32(kf[decltype(P)::v], qf, c); };
        auto merge = [&](const f32x16& a, const f32x16& b) {
            f32x16 o_;
#pragma unroll
            for (int j = 0; j < 16; ++j) { const float x_ = a[j], y_ = b[j]; o_[j] = __builtin_bit_cast(float, (__builtin_bit_cast(unsigned, x_) & mk[j]) | (__builtin_bit_cast(unsigned, y_) & ~mk[j])); }
            return o_;
        };
        float mx = NEG;
        auto red = [&](const f32x16& s_) {
#pragma unroll
            for (int j = 0; j < 16; j += 2) mx = fmaxf(fmaxf(s_[j], s_[j + 1]), mx);
        };
        {
            f32x16 S[4];
            S[0] = score(IC<0>{}, zero); S[1] = score(IC<1>{}, zero); S[2] = score(IC<2>{}, zero); S[3] = score(IC<3>{}, zero);
            SB();
            if constexpr (FHP == 1) { raise(); SB(); }
            red(merge(S[0], S[1]));
            S[0] = score(IC<4>{}, zero); S[1] = score(IC<5>{}, zero);
            SB();
            sfor<2, 9>([&](auto P) {
                constexpr int pp = decltype(P)::v;
                if constexpr (FHP == pp) { raise(); SB(); }
                if constexpr (FHL == pp) ld8c(fhv, voFH, rw, 0u);
                if (isC) { issueFp(pp - 2, 2 * ww, vW, vO); issueFp(pp - 2, 2 * ww + 1, vW, vO); }
                red(S[pp % 4]);
                if constexpr (pp + 4 < 9) S[pp % 4] = score(IC<pp + 4>{}, zero);
                SB();
            });
        }
        {
            const u32x2 sw = __builtin_amdgcn_permlane32_swap(__builtin_bit_cast(unsigned, mx), __builtin_bit_cast(unsigned, mx), false, false);
            const unsigned s0_ = sw[0], s1_ = sw[1];
            mx = fmaxf(__builtin_bit_cast(float, s0_), __builtin_bit_cast(float, s1_));
        }
        f32x16 negm;
#pragma unroll
        for (int j = 0; j < 16; ++j) negm[j] = -mx;
        FST(9);
        f32x16 S[3];
        S[0] = score(IC<0>{}, negm); S[1] = score(IC<1>{}, negm); S[2] = score(IC<2>{}, negm);
        bf16x8 vf[2][2];
        auto rdv = [&](auto P, bf16x8 (&v)[2]) {
            constexpr int pp = decltype(P)::v, tile = (pp + 8) % 9;
            const unsigned dz = tile < t0 ? (unsigned)(L_ZERO - L_GW) : 0u;
            const LAS char* p0 = lp(vb0 + vstep * (unsigned)tile + (blk ? dz : 0u));
            v[0] = cat8(trd(p0), trd(p0 + 256)); v[1] = cat8(trd(p0 + 512), trd(p0 + 768));
        };
        rdv(IC<0>{}, vf[0]); rdv(IC<1>{}, vf[1]);
        if constexpr (FHP == 9) raise();
        if constexpr (FHL == 9) ld8c(fhv, voFH, rw, 0u);
        FST(10);
        auto slot = [&](auto SL) {
            constexpr int sl = decltype(SL)::v;
            if constexpr (sl < 2) sfor<7 + 3 * sl, 10 + 3 * sl>([&](auto C) { issueFp(decltype(C)::v, w, vW, vO); });
            if constexpr (sl == FHL - 10) ld8c(fhv, voFH, rw, 0u);
            if constexpr (sl == FHS) {
                FST(11);
                waitv_tie2<(FHL >= 10 ? 3 * ((FHS < 1 ? FHS : 1) - (FHL - 10) > 0 ? (FHS < 1 ? FHS : 1) - (FHL - 10) : 0) : 3 * (FHS < 2 ? FHS + 1 : 2))>(fhv);
                unsigned spins = 0;
                while (!__all(fhv[0] == tok[0] && fhv[1] == tok[1]) && ++spins < SPIN_CAP) { __builtin_amdgcn_s_sleep(1); ld8c(fhv, voFH, rw, 0u); waitv_tie2<0>(fhv); }
                FST(12);
            }
            if constexpr (sl >= FHS) {
                constexpr int ns = 8 - FHS, c0 = 22 * (sl - FHS) / ns, c1 = 22 * (sl - FHS + 1) / ns;
                sfor<c0, c1>([&](auto C) { constexpr int c = decltype(C)::v; const int cc = ccF(c); ld16c(af[c], l16, rw, (cc == 21 && kq >= 2) ? WS_END : sAf + (unsigned)(4 * cc) * 1024u); });
            }
        };
        auto expack = [&](const f32x16& s_, u32x4& p0, u32x4& p1) {
#pragma unroll
            for (int j = 0; j < 4; ++j) {
                p0[j] = cvtpk(__builtin_amdgcn_exp2f(s_[2 * j]), __builtin_amdgcn_exp2f(s_[2 * j + 1]));
                p1[j] = cvtpk(__builtin_amdgcn_exp2f(s_[8 + 2 * j]), __builtin_amdgcn_exp2f(s_[8 + 2 * j + 1]));
            }
        };
        f32x16 o;
        SB();
        {
            u32x4 p0, p1;
            expack(merge(S[0], S[1]), p0, p1);
            S[0] = score(IC<3>{}, negm); S[1] = score(IC<4>{}, negm);
            u32x4 d0, d1, e0, e1;
#pragma unroll
            for (int j = 0; j < 4; ++j) {
                const unsigned m0 = (mk[2 * j] & 0xFFFFu) | (mk[2 * j + 1] & 0xFFFF0000u), m1 = (mk[8 + 2 * j] & 0xFFFFu) | (mk[8 + 2 * j + 1] & 0xFFFF0000u);
                const unsigned a0 = p0[j], a1 = p1[j];
                d0[j] = a0 & m0; e0[j] = a0 & ~m0; d1[j] = a1 & m1; e1[j] = a1 & ~m1;
            }
            o = MFMA32(vf[0][0], __builtin_bit_cast(bf16x8, d0), zero);
            o = MFMA32(vf[0][1], __builtin_bit_cast(bf16x8, d1), o);
            rdv(IC<2>{}, vf[0]);
            o = MFMA32(vf[1][0], __builtin_bit_cast(bf16x8, e0), o);
            o = MFMA32(vf[1][1], __builtin_bit_cast(bf16x8, e1), o);
            slot(IC<0>{});
            SB();
        }
        sfor<2, 9>([&](auto P) {
            constexpr int pp = decltype(P)::v;
            if constexpr (pp + 1 < 9) rdv(IC<pp + 1>{}, vf[(pp + 1) & 1]);
            u32x4 p0, p1;
            expack(S[pp % 3], p0, p1);
            if constexpr (pp + 3 < 9) S[pp % 3] = score(IC<pp + 3>{}, negm);
            o = MFMA32(vf[pp & 1][0], __builtin_bit_cast(bf16x8, p0), o);
            o = MFMA32(vf[pp & 1][1], __builtin_bit_cast(bf16x8, p1), o);
            slot(IC<pp - 1>{});
            SB();
        });
        const float sc = g / o[8];
        st16c(nosent(pack8(o, 0, sc)), l16, rw, WS_A + (unsigned)((tb * NKS + NKS_H + hp * 2 + nh) * 1024));
    }
    STAMP(5);

    {
        const bool boss = blockIdx.x == 0 && w == 0;
        u32x4 ea[2];
        unsigned woff[2];
#pragma unroll
        for (int t = 0; t < 2; ++t) { const int row = 16 * kq + 4 * h + q; const int c16 = (t * 4 + blk * 2 + (p >> 1)) ^ (((row >> 1) & 1) << 2); woff[t] = (unsigned)(row * 128 + c16 * 16 + (p & 1) * 8); }
        f32x16 acc[2];
#pragma unroll
        for (int i = 0; i < 2; ++i)
#pragma unroll
            for (int j = 0; j < 16; ++j) acc[i][j] = 0.f;
        auto mm = [&](auto C) {
            constexpr int c = decltype(C)::v;
            const LAS char* wb = L + fsl(c);
#pragma unroll
            for (int t = 0; t < 2; ++t) {
                const bf16x8 wf = cat8(trd(wb + woff[t]), trd(wb + woff[t] + 8 * 128));
                acc[t] = MFMA32(wf, __builtin_bit_cast(bf16x8, af[c]), acc[t]);
            }
        };
        auto issueF1 = [&](auto C) { issueFp(decltype(C)::v, w, vW, vO); };
        waitv_bar<(FHS == 0 ? 21 : 23)>();
        STAMP(6);
        if (boss) { ld16c(ea[0], l16, rw, WS_FH); ld16c(ea[1], l16, rw, WS_FH + 1024u); }
        auto ldO = [&](auto C) { constexpr int c = decltype(C)::v; ld16c(af[c], l16, rw, sAf + (unsigned)(NKS_H + 4 * (c - 22)) * 1024u); };
        sfor<0, 6>([&](auto C) { constexpr int c = decltype(C)::v; waitv_tie<22>(af[c]); mm(C); issueF1(IC<13 + c>{}); SB(); });
        FST(29);
        BAR_L();
        FST(30);
        sfor<6, 13>([&](auto C) { constexpr int c = decltype(C)::v; waitv_tie<22>(af[c]); mm(C); if constexpr (c < 12) issueF1(IC<13 + c>{}); else ldO(IC<22>{}); SB(); });
        STAMP(7);
        waitv_bar<7>();
        FST(33);
        sfor<13, 22>([&](auto C) { TIE(af[decltype(C)::v]); });
        bool epoch_done = !boss;
        if (boss) {
            TIE(ea[0]); TIE(ea[1]);
            bool ok = true;
#pragma unroll
            for (int j = 0; j < 2; ++j) { const unsigned a0 = ea[j][0], a1 = ea[j][1], a2 = ea[j][2], a3 = ea[j][3]; ok = ok && a0 == tok[0] && a1 == tok[1] && a2 == tok[0] && a3 == tok[1]; }
            if (__all(ok)) { epoch_done = true; if (lane == 0) { const unsigned long long e1 = epoch + 1; const u32x2 ev1 = {(unsigned)e1, (unsigned)(e1 >> 32)}; st8c(ev1, 0u, rw, WS_E); } }
        }
        sfor<13, 19>([&](auto C) {
            constexpr int c = decltype(C)::v;
            mm(C);
            if constexpr (c < 18) { issueF1(IC<12 + c>{}); ldO(IC<10 + c>{}); } else { ldO(IC<28>{}); ldO(IC<29>{}); }
            SB();
        });
        FST(35);
        waitv_bar<13>();
        FST(36);
        sfor<19, 22>(mm);
        STAMP(8);
        auto validO = [&](auto C0) {
            constexpr int c0 = decltype(C0)::v, c1 = c0 == 22 ? 25 : 30;
            unsigned mxs = 0;
            sfor<c0, c1>([&](auto C) { TIE(af[decltype(C)::v]); mxs = max(mxs, anysent(af[decltype(C)::v])); });
            if (__builtin_expect(!__all(mxs != SENT), 0)) {
                unsigned spins = 0;
                for (;;) {
                    __builtin_amdgcn_s_sleep(1);
                    sfor<c0, 30>(ldO);
                    waitv<0>();
                    mxs = 0;
                    sfor<c0, 30>([&](auto C) { TIE(af[decltype(C)::v]); mxs = max(mxs, anysent(af[decltype(C)::v])); });
                    if (__all(mxs != SENT) || ++spins >= SPIN_CAP) break;
                }
            }
        };
        waitv<8>();
        validO(IC<22>{});
        sfor<22, 25>(mm);
        waitv<0>();
        validO(IC<25>{});
        STAMP(9);
        BAR_L();
        constexpr int RP = 144, RB0 = 16 * KB, RB1 = 80 * KB;
        LAS char* dst = L + kq * (64 * RP) + (fm * 32 + r) * RP + 16 * h;
        auto mt = [&](auto C, auto T) {
            constexpr int c = decltype(C)::v, t = decltype(T)::v;
            const LAS char* wb = L + fsl(c);
            const bf16x8 wf = cat8(trd(wb + woff[t]), trd(wb + woff[t] + 8 * 128));
            acc[t] = MFMA32(wf, __builtin_bit_cast(bf16x8, af[c]), acc[t]);
        };
        auto wrp = [&](int t, int g4) { *(LAS f32x4*)(dst + (t ? RB1 : RB0) + g4 * 32) = (f32x4){acc[t][4 * g4], acc[t][4 * g4 + 1], acc[t][4 * g4 + 2], acc[t][4 * g4 + 3]}; };
        sfor<25, 30>([&](auto C) { mt(C, IC<0>{}); });
        SB();
        sfor<25, 30>([&](auto C) { constexpr int c = decltype(C)::v; mt(C, IC<1>{}); if constexpr (c > 25) wrp(0, c - 26); SB(); });
        STAMP(10);
#pragma unroll
        for (int g4 = 0; g4 < 4; ++g4) wrp(1, g4);
        BAR_L();
        {
            const int tok_ = tid >> 3, oc = tid & 7;
            f32x4 a = {0.f, 0.f, 0.f, 0.f}, b = a;
#pragma unroll
            for (int k = 0; k < 4; ++k) {
                const LAS char* src = L + ((oc >> 2) ? RB1 : RB0) + k * (64 * RP) + tok_ * RP + (oc & 3) * 32;
                a += *(const LAS f32x4*)src; b += *(const LAS f32x4*)(src + 16);
            }
            u32x4 o = {cvtpk(a[0], a[1]), cvtpk(a[2], a[3]), cvtpk(b[0], b[1]), cvtpk(b[2], b[3])};
            st16c(o, (unsigned)(((rbF * 128 + rh * 64 + tok_) * 512 + ct * 64 + oc * 8) * 2), mkrs(out, S_ * D_ * 2), 0u);
        }
        if (!epoch_done) {
            unsigned spins = 0;
            for (;;) {
                ld16c(ea[0], l16, rw, WS_FH); ld16c(ea[1], l16, rw, WS_FH + 1024u); waitv<0>(); TIE(ea[0]); TIE(ea[1]);
                bool ok = true;
#pragma unroll
                for (int j = 0; j < 2; ++j) { const unsigned a0 = ea[j][0], a1 = ea[j][1], a2 = ea[j][2], a3 = ea[j][3]; ok = ok && a0 == tok[0] && a1 == tok[1] && a2 == tok[0] && a3 == tok[1]; }
                if (__all(ok) || ++spins >= SPIN_CAP) break;
                __builtin_amdgcn_s_sleep(2);
            }
            if (lane == 0) { const unsigned long long e1 = epoch + 1; const u32x2 ev1 = {(unsigned)e1, (unsigned)(e1 >> 32)}; st8c(ev1, 0u, rw, WS_E); }
        }
    }
    asm volatile(".Lpf_end:");
}

extern "C" void kernel_launch(void* const* d_in, const int* in_sizes, int n_in, void* d_out, int out_size, void* d_ws, size_t ws_size, hipStream_t stream) {
    (void)hipFuncSetAttribute((const void*)k_fused, hipFuncAttributeMaxDynamicSharedMemorySize, LDS_BYTES);
    k_fused<<<256, 512, LDS_BYTES, stream>>>(d_in[0], d_in[1], d_in[2], d_in[4], d_in[5], d_in[6], (char*)d_ws, d_out);
}
```

```cpp
#include <hip/hip_runtime.h>
#include <hip/hip_bf16.h>
#include <stdint.h>

#define DI __device__ __forceinline__
#define LAS __attribute__((address_space(3)))
#define GAS __attribute__((address_space(1)))
typedef short bf16x8 __attribute__((ext_vector_type(8)));
typedef short s16x4 __attribute__((ext_vector_type(4)));
typedef float f32x16 __attribute__((ext_vector_type(16)));
typedef float f32x4 __attribute__((ext_vector_type(4)));
typedef float f32x2 __attribute__((ext_vector_type(2)));
typedef __bf16 bf16x2_t __attribute__((ext_vector_type(2)));
typedef unsigned u32x4 __attribute__((ext_vector_type(4)));
typedef unsigned u32x2 __attribute__((ext_vector_type(2)));
typedef __amdgpu_buffer_rsrc_t rsrc_t;

constexpr int S_ = 2048, D_ = 512, DFF = 1376;
constexpr int NKS_H = DFF / 16;
constexpr int NKS = NKS_H + 32;
constexpr unsigned WS_E = 0;
constexpr unsigned WS_FH = 8192;
constexpr unsigned WS_FK = 16384;
constexpr unsigned WS_FR = 32768;
constexpr unsigned WS_K = 1u << 20;
constexpr unsigned WS_V = WS_K + (2u << 20);
constexpr unsigned WS_A = WS_V + (2u << 20);
constexpr unsigned WS_END = WS_A + 64u * NKS * 1024u;
constexpr unsigned WS_DBG = WS_END;
constexpr unsigned SENT = 0xFFFFFFFFu;
constexpr unsigned SPIN_CAP = 1u << 18;

constexpr float QSCALE = 0.25f * 1.4426950408889634f;

#define MFMA32(a, b, c) __builtin_amdgcn_mfma_f32_32x32x16_bf16((a), (b), (c), 0, 0, 0)

DI unsigned cvtpk(float lo, float hi) { f32x2 v = {lo, hi}; bf16x2_t b = __builtin_convertvector(v, bf16x2_t); return __builtin_bit_cast(unsigned, b); }
DI u32x4 pack8(const f32x16& x, int s, float sc) {
    u32x4 p;
    p[0] = cvtpk(x[8 * s + 0] * sc, x[8 * s + 1] * sc); p[1] = cvtpk(x[8 * s + 2] * sc, x[8 * s + 3] * sc);
    p[2] = cvtpk(x[8 * s + 4] * sc, x[8 * s + 5] * sc); p[3] = cvtpk(x[8 * s + 6] * sc, x[8 * s + 7] * sc);
    return p;
}
DI u32x4 nosent(u32x4 p) {
#pragma unroll
    for (int j = 0; j < 4; ++j) { const unsigned v = p[j]; p[j] = v == SENT ? 0x7FC07FC0u : v; }
    return p;
}
DI void dma16(unsigned lds_dst, unsigned voff, rsrc_t rs, unsigned soff) {
    asm volatile("s_mov_b32 m0, %0\n\ts_nop 0\n\tbuffer_load_dwordx4 %1, %2, %3 offen lds" ::"s"(lds_dst), "v"(voff), "s"(rs), "s"(soff) : "memory");
}
DI void ld16c(u32x4& v, unsigned voff, rsrc_t rs, unsigned soff) {
    asm volatile("buffer_load_dwordx4 %0, %1, %2, %3 offen sc1" : "=&v"(v) : "v"(voff), "s"(rs), "s"(soff) : "memory");
}
DI void ld8c(u32x2& v, unsigned voff, rsrc_t rs, unsigned soff) {
    asm volatile("buffer_load_dwordx2 %0, %1, %2, %3 offen sc1" : "=&v"(v) : "v"(voff), "s"(rs), "s"(soff) : "memory");
}
DI void st16c(u32x4 v, unsigned voff, rsrc_t rs, unsigned soff) {
    asm volatile("buffer_store_dwordx4 %0, %1, %2, %3 offen sc1\n\ts_nop 1" ::"v"(v), "v"(voff), "s"(rs), "s"(soff) : "memory");
}
DI void st8c(u32x2 v, unsigned voff, rsrc_t rs, unsigned soff) {
    asm volatile("buffer_store_dwordx2 %0, %1, %2, %3 offen sc1" ::"v"(v), "v"(voff), "s"(rs), "s"(soff) : "memory");
}
template <int N> DI void waitv() { asm volatile("s_waitcnt vmcnt(%0)" ::"n"(N) : "memory"); }
template <int N> DI void waitv_bar() { asm volatile("s_waitcnt vmcnt(%0) lgkmcnt(0)\n\ts_barrier" ::"n"(N) : "memory"); }
DI void ld_epoch(unsigned long long& e, const char* ws) { asm volatile("s_load_dwordx2 %0, %1, 0x0" : "=s"(e) : "s"(ws) : "memory"); }
template <int N> DI void waitv_bar_e(unsigned long long& e) { asm volatile("s_waitcnt vmcnt(%1) lgkmcnt(0)\n\ts_barrier" : "+s"(e) : "n"(N) : "memory"); }
template <int N> DI void waitv_tie(u32x4& a) { asm volatile("s_waitcnt vmcnt(%1)" : "+v"(a) : "n"(N) : "memory"); }
template <int N> DI void waitv_tie2(u32x2& a) { asm volatile("s_waitcnt vmcnt(%1)" : "+v"(a) : "n"(N) : "memory"); }
DI void TIE(u32x4& x) { asm volatile("" : "+v"(x)); }
DI void TIE(u32x2& x) { asm volatile("" : "+v"(x)); }
#define BAR_L() asm volatile("s_waitcnt lgkmcnt(0)\n\ts_barrier" ::: "memory")
DI s16x4 trd(const LAS char* p) { return __builtin_bit_cast(s16x4, __builtin_amdgcn_ds_read_tr16_b64_v4i16((LAS s16x4*)p)); }
DI bf16x8 cat8(s16x4 lo, s16x4 hi) { return __builtin_shufflevector(lo, hi, 0, 1, 2, 3, 4, 5, 6, 7); }
DI rsrc_t mkrs(const void* p, unsigned bytes) { return __builtin_amdgcn_make_buffer_rsrc((void*)p, 0, (int)bytes, 0x00020000); }
DI unsigned umax3(unsigned a, unsigned b, unsigned c) { return max(max(a, b), c); }
DI unsigned anysent(const u32x4& a) { const unsigned a0 = a[0], a1 = a[1], a2 = a[2], a3 = a[3]; return max(max(a0, a1), max(a2, a3)); }

#define STAMP(i) do { } while (0)
#define STAMPC(i) do { } while (0)
#define STAMPW 0
#define FST(i) do { } while (0)

template <int I> struct IC { static constexpr int v = I; };
template <int B, int E, class F> DI void sfor(F&& f) { if constexpr (B < E) { f(IC<B>{}); sfor<B + 1, E>(f); } }

DI void block_map(int b, int& rb, int& hp) { const int xg = b & 7, idx = b >> 3; rb = (xg >> 2) * 8 + (idx >> 2); hp = (xg & 3) * 4 + (idx & 3); }

constexpr int KB = 1024;
__host__ __device__ constexpr int stS(int g) { return (g == 3 ? 0 : g) * 48 * KB; }
constexpr int L_W1 = 48 * KB;
constexpr int L_WQ = 0;
__host__ __device__ constexpr int fsl(int c) {
    constexpr int t[19] = {32, 40, 144, 48, 56, 64, 72, 128, 136, 0, 8, 16, 24, 80, 88, 96, 104, 112, 120};
    return t[c % 19] * KB;
}
constexpr int L_KST = 80 * KB, L_VST = 104 * KB;
constexpr int L_MISC = 152 * KB;
constexpr int L_GW = L_MISC, L_GP = L_MISC + 1024, L_QX = L_MISC + 2048, L_ONE = L_MISC + 6144, L_CNT = L_MISC + 6400, L_CNT2 = L_MISC + 6464, L_ZERO = L_MISC + 6656, L_DUM = L_MISC + 7680;
constexpr int LDS_BYTES = 163840;
#define KVFLAG 12
#define FHS 2
#define FHP 1
#define FHL 10
#define KVFI 16
#define OUTM 1
#define FROT 0
#define KVM 44

#define PFC 6
#define PFCW 7
#define ROT (((idx & 3) + (idx >> 2)) & 3)
#define NPF 1
#define PFAW 4
#define PFB 2
#define PFWC 0
#define BAR_N(n) asm volatile("s_waitcnt lgkmcnt(" #n ")\n\ts_barrier" ::: "memory")
#define SB() __builtin_amdgcn_sched_barrier(0)
typedef short s16x2 __attribute__((ext_vector_type(2)));
DI unsigned relu2(unsigned v) { const s16x2 z = {0, 0}; return __builtin_bit_cast(unsigned, __builtin_elementwise_max(__builtin_bit_cast(s16x2, v), z)); }
DI const LAS char* lp(unsigned a) { return (const LAS char*)(uintptr_t)a; }
DI unsigned opq(unsigned v) { asm volatile("" : "+v"(v)); return v; }

__global__ __launch_bounds__(512, 2) void k_fused(const void* x, const void* wqkv, const void* wo, const void* w1, const void* w2, const void* gw, char* ws, void* out) {
    extern __shared__ __attribute__((aligned(16))) char smem[];
    LAS char* L = (LAS char*)smem;
    const int tid = threadIdx.x, lane = tid & 63;
    int w = __builtin_amdgcn_readfirstlane(tid >> 6);
    asm volatile("" : "+s"(w));
    const bool isC = w < 4;
    const int ww = w & 3, r = lane & 31, h = lane >> 5;
    int rb, hp; block_map(blockIdx.x, rb, hp);
    const int xg = blockIdx.x & 7, idx = blockIdx.x >> 3;
#define CTM 2
    const int ct = ((xg >> CTM) << CTM) | (idx & ((1 << CTM) - 1)), fr_ = ((xg & ((1 << CTM) - 1)) << (5 - CTM)) | (idx >> CTM), rbF = fr_ >> 1, rh = fr_ & 1;
    const unsigned ldsb = (unsigned)(uintptr_t)L;
    auto touch = [&](const char* a) { asm volatile("s_mov_b32 m0, %0\n\ts_nop 0\n\tglobal_load_lds_dword %1, off" ::"s"(ldsb + (unsigned)L_DUM), "v"(a) : "memory"); };
    asm volatile(".Lpf_start:");
    STAMP(0); STAMPC(12);
    if (w == PFCW) {
        unsigned long long pc0; unsigned clen, cback;
        asm volatile("s_getpc_b64 %0\n.Lpf_here:\n\ts_mov_b32 %1, .Lpf_end-.Lpf_start-4\n\ts_mov_b32 %2, .Lpf_here-.Lpf_start" : "=s"(pc0), "=s"(clen), "=s"(cback));
        const unsigned oc = min((unsigned)((idx * PFC + (lane < PFC ? lane : 0)) * 128), clen) & ~3u;
        const char* a = (const char*)(pc0 - cback) + oc;
        touch(a);
    }
    unsigned long long epoch;
    const rsrc_t rx = mkrs(x, S_ * D_ * 2), rq = mkrs(wqkv, D_ * 3 * D_ * 2), r1 = mkrs(w1, D_ * DFF * 2), rg = mkrs(gw, D_ * 2);
    const rsrc_t r2 = mkrs(w2, DFF * D_ * 2), ro = mkrs(wo, D_ * D_ * 2), rw = mkrs(ws, WS_END);
    const unsigned l16 = (unsigned)lane * 16u;
    const int tb = rb * 4 + ww;
    const int q = (lane & 15) >> 2, p = lane & 3, blk = (lane >> 4) & 1;
    const int u0 = (hp * 3 < 40) ? hp * 3 : 40;
    const int row8 = lane >> 3, pos = lane & 7;
    const u32x4 sentv = {SENT, SENT, SENT, SENT};

    const unsigned voX = (unsigned)((8 * ww + row8) * 1024 + (pos ^ (((8 * ww + row8) >> 1) & 7)) * 16);
    const unsigned sX = (unsigned)rb * 128u * 1024u;
    const int rot = ROT;
    auto issueX = [&](int g) {
#pragma unroll
        for (int t = 0; t < 8; ++t)
            dma16(ldsb + (unsigned)(g * 48 * KB + (t >> 2) * 16384 + (t & 3) * 4096) + (unsigned)ww * 1024u, voX, rx, sX + (unsigned)(((g + rot) & 3) * 256 + (t & 3) * 32768 + (t >> 2) * 128));
    };
    auto issueX3 = [&]() {
#pragma unroll
        for (int t = 0; t < 8; ++t)
            dma16(ldsb + (unsigned)((t >> 2) * 16384 + (t & 3) * 4096) + (unsigned)ww * 1024u, voX, rx, sX + (unsigned)(((3 + rot) & 3) * 256 + (t & 3) * 32768 + (t >> 2) * 128));
    };
    const unsigned voKV = (unsigned)((16 * ww + row8) * 3072 + 1024 + (hp & 1) * 1024 + (hp >> 1) * 128 + (pos ^ (((row8 >> 1) & 1) << 2)) * 16);
    auto issueKV = [&](int j) {
#pragma unroll
        for (int t = 0; t < 2; ++t) dma16(ldsb + (unsigned)(((j >> 1) == 3 ? 0 : (j >> 1)) * 48 * KB + 32 * KB + (j & 1) * 8192 + t * 1024) + (unsigned)ww * 2048u, voKV, rq, (unsigned)(((j + 2 * rot) & 7) * 64 + 8 * t) * 3072u);
    };
    auto mkvoW = [&](int ln) { const int r8 = ln >> 3, ps = ln & 7; return (unsigned)(r8 * 1024 + ct * 128 + (ps ^ (((r8 >> 1) & 1) << 2)) * 16); };
    auto mkvoO = [&](int ln) { const int r8 = ln >> 3, ps = ln & 7; return (unsigned)((8 * (r8 >> 2) + (r8 & 3)) * 1024 + ct * 128 + (ps ^ (((r8 >> 1) & 1) << 2)) * 16); };
    const int rotF = FROT;
    auto ccF = [&](int c) { const int t = c + rotF; return t >= 22 ? t - 22 : t; };
    auto issueFp = [&](int c, int pi, unsigned vW, unsigned vO) {
        const unsigned dst = ldsb + (unsigned)fsl(c) + (unsigned)pi * 1024u;
        if (c < 22) dma16(dst, vW, r2, (unsigned)(ccF(c) * 65536) + (unsigned)pi * 8192u);
        else dma16(dst, vO, ro, (unsigned)((c - 22) * 65536) + (unsigned)(16 * (pi >> 1) + 4 * (pi & 1)) * 1024u);
    };

    unsigned long long tok64 = 0;
    u32x2 tok;
    bf16x8 qf;
    auto mktok = [&]() {
        tok64 = epoch * 0x9E3779B97F4A7C15ull + 0x632BE59BD9B4E019ull;
        tok = (u32x2){(unsigned)tok64, (unsigned)(tok64 >> 32)};
    };

    const int hc = isC ? 0 : 1;
    auto issueHalf = [&](int g) {
        issueKV(2 * g + hc);
#pragma unroll
        for (int t = 0; t < 4; ++t)
            dma16(ldsb + (unsigned)(g * 48 * KB + t * 4096) + (unsigned)(hc * 16384 + ww * 1024), voX, rx, sX + (unsigned)(((g + rot) & 3) * 256 + t * 32768) + (unsigned)hc * 128u);
    };
    auto pfAddrA = [&]() -> const char* {
        const int g4 = xg & 3, r0 = 8 * ((xg >> 2) * 32 + idx) + 2 * ww, l = lane < 18 ? lane : 0;
        const int rr = l >= 7 ? 1 : 0, c = l - 7 * rr;
        const unsigned o1 = min((unsigned)((((r0 + rr) * 2752 + g4 * 768) & ~127) + c * 128), (unsigned)(D_ * DFF * 2 - 4));
        const unsigned oq = (unsigned)((r0 + ((l >> 1) & 1)) * 3072 + g4 * 256 + (l & 1) * 128);
        return l < 14 ? (const char*)w1 + o1 : (const char*)wqkv + oq;
    };
    auto pfAddrB = [&]() -> const char* {
        const int l = (lane & 31) < 15 ? (lane & 31) : 0;
        const int gr = fr_ * 59 + min(ww * 15 + l, 58);
        return gr < DFF ? (const char*)w2 + (unsigned)((gr * 8 + ct) * 128) : (const char*)wo + (unsigned)(((gr - DFF) * 8 + ct) * 128);
    };
    auto pfA = [&]() { touch((PFB == 0 && lane >= 32) ? pfAddrB() : pfAddrA()); };
    auto pfB = [&]() { touch(pfAddrB()); };
    if (w == 0) dma16(ldsb + L_GW, (l16 + (unsigned)rot * 256u) & 1023u, rg, 0u);
    issueHalf(0);
    FST(26);
    if (PFAW < 4 ? isC : !isC) pfA();
    FST(27);
    issueHalf(1);
    FST(15);
    constexpr int RP = 144, RB0 = 16 * KB, RB1 = 80 * KB + 128;
    const int l5 = lane & 31, gsel = (0xF00F0FF0u >> l5) & 1;
    const int gi = l5 - (l5 < 4 ? 0 : l5 < 12 ? 4 : l5 < 20 ? 8 : l5 < 28 ? 12 : 16);
    const int tok_ = w * 8 + (lane >> 5) * 4 + gsel * 2 + (gi >> 3), oc = gi & 7;
    const unsigned rsrc_ = opq(ldsb + (unsigned)(((oc >> 2) ? RB1 : RB0) + tok_ * RP + (oc & 3) * 32));
    const unsigned voOut = opq((unsigned)(((rbF * 128 + rh * 64 + tok_) * 512 + ct * 64 + oc * 8) * 2));
    if (isC) {
        const unsigned sKV = ((hp & 1) ? WS_V : WS_K) + (unsigned)(((hp & ~1) * 64 + tb) * 2048);
        unsigned long long ep; ld_epoch(ep, ws);
        const int xrow = ww * 32 + r;
        unsigned xoff[4];
#pragma unroll
        for (int s = 0; s < 4; ++s) { const int c = 2 * s + h; xoff[s] = ldsb + (unsigned)(xrow * 128 + ((c ^ ((xrow >> 1) & 7)) * 16)); }
        const unsigned kvoff = (unsigned)((8 * h + q) * 128 + ((blk * 2 + (p >> 1)) ^ (((q >> 1) & 1) << 2)) * 16 + (p & 1) * 8);
        const unsigned gwoff = ldsb + (((lane & 15) == blk) ? (unsigned)(L_GW + h * 16) : (unsigned)L_ZERO);
        bf16x8 xf[32];
        f32x16 acc[2];
#pragma unroll
        for (int j = 0; j < 16; ++j) { acc[0][j] = 0.f; acc[1][j] = 0.f; }
        f32x4 ag = {0.f, 0.f, 0.f, 0.f};
        FST(13);
        waitv_bar_e<(PFAW < 4 ? 6 + NPF : 6)>(ep);
        STAMP(1);
        epoch = ep; mktok();
        sfor<0, 4>([&](auto G) {
            constexpr int g = decltype(G)::v;
            const unsigned hi = stS(g) >= 65536 ? 65536u : 0u;
#pragma unroll
            for (int c = 0; c < 2; ++c)
#pragma unroll
                for (int s = 0; s < 4; ++s) xf[g * 8 + c * 4 + s] = *(const LAS bf16x8*)(lp((stS(g) >= 65536 ? opq(xoff[s] + hi) : xoff[s])) + (stS(g) - (int)hi + c * 16384));
            const unsigned kb0 = opq(ldsb + kvoff + (unsigned)(stS(g) + 32 * KB)), kb1 = opq(ldsb + (kvoff ^ 64u) + (unsigned)(stS(g) + 32 * KB));
            constexpr int DK = 4;
            bf16x8 wr[DK + 1], gf[4];
            sfor<0, 16 + DK>([&](auto I) {
                constexpr int i = decltype(I)::v;
                if constexpr (i < 16) {
                    const LAS char* pw = lp((i & 1) ? kb1 : kb0) + (i >> 3) * 8192 + ((i >> 1) & 3) * 2048;
                    wr[i % (DK + 1)] = cat8(trd(pw), trd(pw + 512));
                    if constexpr ((i & 1) == 0) gf[(i >> 1) & 3] = *(const LAS bf16x8*)(lp(gwoff) + (g * 128 + (i >> 1) * 16) * 2);
                }
                if constexpr (i >= DK) {
                    constexpr int j = i - DK;
                    acc[j & 1] = MFMA32(wr[j % (DK + 1)], xf[g * 8 + (j >> 1)], acc[j & 1]);
                    if constexpr ((j & 1) == 1) ag = __builtin_amdgcn_mfma_f32_16x16x32_bf16(gf[(j >> 1) & 3], xf[g * 8 + (j >> 1)], ag, 0, 0, 0);
                }
                SB();
            });
            FST(g);
            if constexpr (g == 0) waitv_bar<0>(); else if constexpr (g < 3) BAR_L();
        });
        STAMP(2);
        if (lane < 16) {
            ((LAS float*)(L + L_GP))[ww * 32 + lane] = 1.f / (1.f + __expf(-ag[0]));
            ((LAS float*)(L + L_GP))[ww * 32 + 16 + lane] = 1.f / (1.f + __expf(-ag[1]));
        }
        const float og = 1.f - ((const LAS float*)(L + L_GP))[xrow];
        const unsigned ql = (hp & 1) ? (unsigned)(2 * r + h) * 16u : l16;
        const unsigned w1off = ldsb + (unsigned)((8 * h + q) * 192 + blk * 32 + p * 8);
        const unsigned w1a = opq(w1off + (unsigned)L_W1), w1b = opq(w1off + (unsigned)(L_W1 + 60 * KB));
        const unsigned wqo = opq(ldsb + (unsigned)(L_WQ + (8 * h + q) * 64 + blk * 32 + p * 8));
        const unsigned sA = WS_A + (unsigned)(tb * NKS + 2 * u0) * 1024u;
        const int dup = hp * 3 - u0;
#pragma unroll
        for (int n = 0; n < 4; ++n) st16c(pack8(acc[n >> 1], n & 1, 1.f), ql, rw, sKV + (unsigned)((n >> 1) * 131072 + (n & 1) * 1024));
        BAR_L();
        FST(4);
        {
            constexpr int DP = 6, RP_ = 8;
            bf16x8 wr[RP_];
            f32x16 ah[2];
            u32x4 pk;
            unsigned wql = 0;
            sfor<0, 128 + DP>([&](auto I) {
                constexpr int i = decltype(I)::v;
                if constexpr (i == KVFI) {
                    FST(20);
                    waitv<0>();
                    FST(21);
                    if (lane == 0) st8c(tok, 0u, rw, WS_FK + (unsigned)(((hp >> 1) * 64 + tb) * 2 + (hp & 1)) * 8u);
                }
                if constexpr (i == 16) { FST(5); BAR_N(12); FST(6); }
                if constexpr (i == KVM || i == 96) {
                    asm volatile("" ::: "memory");
                    if (lane == 0) __hip_atomic_fetch_add((LAS unsigned*)(L + L_CNT2 + (i == KVM ? 0 : 4)), 1u, __ATOMIC_RELAXED, __HIP_MEMORY_SCOPE_WORKGROUP);
                    asm volatile("" ::: "memory");
                    FST(i == KVM ? 7 : 8);
                }
                if constexpr (i == 90) wql = __hip_atomic_load((LAS unsigned*)(L + L_CNT2 + 12), __ATOMIC_RELAXED, __HIP_MEMORY_SCOPE_WORKGROUP);
                if constexpr (i == 96) {
                    if (__builtin_expect(__builtin_amdgcn_readfirstlane(wql) < 4u, 0)) {
                        unsigned spins = 0;
                        do { __builtin_amdgcn_s_sleep(1); wql = __hip_atomic_load((LAS unsigned*)(L + L_CNT2 + 12), __ATOMIC_RELAXED, __HIP_MEMORY_SCOPE_WORKGROUP); } while (__builtin_amdgcn_readfirstlane(wql) < 4u && ++spins < SPIN_CAP);
                    }
                    asm volatile("" ::: "memory");
                    FST(23);
                }
                if constexpr (i < 96) {
                    constexpr int t = i / 32, c = (i % 32) / 4, s = i % 4;
                    const LAS char* pw = lp(c < 5 ? w1a : w1b) + ((c < 5 ? c : c - 5) * 12 * KB + s * 16 * 192 + t * 64);
                    wr[i % RP_] = cat8(trd(pw), trd(pw + 4 * 192));
                } else if constexpr (i < 128) {
                    const LAS char* pw = lp(wqo) + (i - 96) * 1024;
                    wr[i % RP_] = cat8(trd(pw), trd(pw + 256));
                }
                if constexpr (i >= DP) {
                    constexpr int j = i - DP, T = j / 32, jj = j % 32;
                    if constexpr (jj == 0) {
                        f32x16 z;
#pragma unroll
                        for (int e = 0; e < 16; ++e) z[e] = 0.f;
                        ah[T & 1] = MFMA32(wr[j % RP_], xf[jj], z);
                    } else ah[T & 1] = MFMA32(wr[j % RP_], xf[jj], ah[T & 1]);
                    if constexpr (T >= 1 && jj >= 1 && jj < 9) {
                        constexpr int n = (jj - 1) / 4, qd = (jj - 1) % 4, t = T - 1;
                        pk[qd] = relu2(cvtpk(ah[t & 1][8 * n + 2 * qd] * og, ah[t & 1][8 * n + 2 * qd + 1] * og));
                        if constexpr (qd == 3) { if (t >= dup) st16c(pk, l16, rw, sA + (unsigned)((2 * t + n) * 1024)); }
                    }
                }
                SB();
            });
            qf = __builtin_bit_cast(bf16x8, pack8(ah[1], 0, QSCALE));
            *(LAS u32x4*)(L + L_QX + ww * 1024 + l16) = pack8(ah[1], 1, QSCALE);
        }
        STAMP(3);
        BAR_L();
    } else {
        unsigned long long ep; ld_epoch(ep, ws);
        if (w == 5 && lane == 0) { *(LAS unsigned*)(L + L_CNT) = 0u; *(LAS unsigned*)(L + L_CNT2) = 0u; *(LAS unsigned*)(L + L_CNT2 + 4) = 0u; *(LAS unsigned*)(L + L_CNT2 + 8) = 0u; *(LAS unsigned*)(L + L_CNT2 + 12) = 0u; }
        if (w == 6) *(LAS u32x4*)(L + L_ZERO + l16) = (u32x4){0u, 0u, 0u, 0u};
        unsigned voW1[3];
#pragma unroll
        for (int t = 0; t < 3; ++t) { const int li = 64 * t + lane, row = li / 12, c = li - row * 12; voW1[t] = (unsigned)((16 * ww + row) * 2752 + u0 * 64 + c * 16); }
        const unsigned voQ = (unsigned)((32 * ww + (lane >> 2)) * 3072 + hp * 64 + (lane & 3) * 16);
        auto issueW1 = [&](int c) {
#pragma unroll
            for (int t = 0; t < 3; ++t) dma16(ldsb + (unsigned)(L_W1 + c * 12 * KB + t * 1024) + (unsigned)ww * 3072u, voW1[t], r1, (unsigned)((c + 2 * rot) & 7) * 64u * 2752u);
        };
        auto issueWQ = [&](int pc) {
#pragma unroll
            for (int t = 0; t < 2; ++t) dma16(ldsb + (unsigned)(L_WQ + pc * 8192 + t * 1024) + (unsigned)ww * 2048u, voQ, rq, (unsigned)(((pc + rot) & 3) * 128 + 16 * t) * 3072u);
        };
        const unsigned vW = mkvoW(lane);
        auto issueF2 = [&](int c) { issueFp(c, 2 * ww, vW, 0u); issueFp(c, 2 * ww + 1, vW, 0u); };
        FST(13);
        waitv_bar_e<(PFAW >= 4 ? 6 + NPF : 6)>(ep);
        STAMP(1);
        epoch = ep; mktok();
        issueX(2); issueKV(4); issueKV(5);
        if (PFB == 1) pfB();
        waitv_bar<(PFB == 1 ? 12 + NPF : 12)>();
        FST(0);
        issueX3(); issueKV(6); issueKV(7);
        if (PFB == 2) pfB();
        waitv_bar<(PFB == 2 ? 12 + NPF : 12)>();
        FST(1);
        issueW1(0); issueW1(1); issueW1(2); issueW1(3);
        if (PFB == 3) pfB();
        waitv_bar<(PFB == 3 ? 12 + NPF : 12)>();
        FST(2);
        issueW1(4); issueW1(5); issueW1(6); issueW1(7);
        STAMP(2);
        waitv_bar<12>();
        FST(4);
        if (w == 4) *(LAS u32x4*)(L + L_GW + l16) = (u32x4){0x3f803f80u, 0x3f803f80u, 0x3f803f80u, 0x3f803f80u};
        issueWQ(0); issueWQ(1); issueWQ(2); issueWQ(3);
        {
            const unsigned sO = WS_A + (unsigned)((tb * NKS + NKS_H + hp * 2) * 1024);
            st16c(sentv, l16, rw, sO); st16c(sentv, l16, rw, sO + 1024u);
        }
        FST(5);
        waitv_bar<10>();
        FST(6);
        auto waitC = [&](int n) {
            unsigned spins = 0;
            while (__hip_atomic_load((LAS unsigned*)(L + L_CNT2 + 4 * n), __ATOMIC_RELAXED, __HIP_MEMORY_SCOPE_WORKGROUP) < 4u && ++spins < SPIN_CAP) __builtin_amdgcn_s_sleep(1);
            asm volatile("" ::: "memory");
        };
        waitv<0>();
        if (lane == 0) __hip_atomic_fetch_add((LAS unsigned*)(L + L_CNT2 + 12), 1u, __ATOMIC_RELAXED, __HIP_MEMORY_SCOPE_WORKGROUP);
        FST(22);
        {
            unsigned spins = 0;
            if (w == 4) {
                unsigned voFK;
                { const int l = lane & 31, i = l < 24 ? l >> 1 : 0, kt = rb * 4 - 8 + i;
                  voFK = WS_FK + (unsigned)(((hp >> 1) * 64 + (kt < 0 ? 0 : kt)) * 2 + (l & 1)) * 8u; }
                u32x2 fkv;
                waitC(0);
                FST(20);
                do { ld8c(fkv, voFK, rw, 0u); waitv_tie2<0>(fkv); } while (!__all(fkv[0] == tok[0] && fkv[1] == tok[1]) && ++spins < SPIN_CAP);
                if (lane == 0) __hip_atomic_store((LAS unsigned*)(L + L_CNT2 + 8), 1u, __ATOMIC_RELAXED, __HIP_MEMORY_SCOPE_WORKGROUP);
            } else {
                while (__hip_atomic_load((LAS unsigned*)(L + L_CNT2 + 8), __ATOMIC_RELAXED, __HIP_MEMORY_SCOPE_WORKGROUP) == 0u && ++spins < SPIN_CAP) __builtin_amdgcn_s_sleep(1);
            }
            asm volatile("" ::: "memory");
        }
        FST(24);
        {
            u32x4 kst[6], vst[6];
            const int hh = ww & 1;
#pragma unroll
            for (int t = 0; t < 6; ++t) {
                const int i = (ww >> 1) + 2 * t, kt = rb * 4 - 8 + i;
                const unsigned sst = (unsigned)(((hp * 64 + (kt < 0 ? 0 : kt)) * 2 + hh) * 1024);
                ld16c(kst[t], kt < 0 ? (unsigned)(lane & 32) * 16u : l16, rw, WS_K + sst); ld16c(vst[t], l16, rw, WS_V + sst);
            }
            FST(7);
            waitC(1);
            FST(21);
            sfor<0, 6>([&](auto T) {
                constexpr int t = decltype(T)::v;
                const int i = (ww >> 1) + 2 * t, kt = rb * 4 - 8 + i;
                waitv_tie<11 - 2 * t>(kst[t]);
                *(LAS u32x4*)(L + L_KST + (hh * 12 + i) * 1024 + l16) = kst[t];
                waitv_tie<10 - 2 * t>(vst[t]);
                *(LAS u32x4*)(L + L_VST + (hh * 12 + i) * 1024 + l16) = kt < 0 ? (u32x4){0u, 0u, 0u, 0u} : vst[t];
            });
        }
        STAMP(3);
        BAR_L();
    }
    STAMP(4);

    int lane2 = lane;
    asm volatile("" : "+v"(lane2));
    const unsigned vW = mkvoW(lane2), vO = mkvoO(lane2);
    const int fm = w & 1, kq = w >> 1;
    const unsigned sAf = WS_A + (unsigned)((rbF * 4 + rh * 2 + fm) * NKS + kq) * 1024u;
    u32x4 af[30];
    {
        const int nh = w >> 2, m = ww;
        const LAS char* kb = L + L_KST + (nh * 12 + m) * 1024 + l16;
        bf16x8 kf[9];
#pragma unroll
        for (int i = 0; i < 9; ++i) kf[i] = *(const LAS bf16x8*)(kb + ((i + 8) % 9) * 1024);
        if (!isC) qf = *(const LAS bf16x8*)(L + L_QX + m * 1024 + l16);
        const float g = ((const LAS float*)(L + L_GP))[m * 32 + r];
        const unsigned voFH = WS_FR + (unsigned)((rbF * 16 + rh * 8 + ct) * 16 + (lane2 & 15)) * 8u;
        u32x2 fhv;
        auto raise = [&]() {
            if (isC) {
                waitv<0>();
                unsigned cnt = 0;
                if (lane == 0) cnt = __hip_atomic_fetch_add((LAS unsigned*)(L + L_CNT), 1u, __ATOMIC_RELAXED, __HIP_MEMORY_SCOPE_WORKGROUP);
                cnt = __builtin_amdgcn_readfirstlane(cnt);
                if (cnt == 3u && lane2 < 17) st8c(tok, lane2 < 16 ? WS_FR + (unsigned)((rb * 16 + lane2) * 16 + hp) * 8u : WS_FH + (unsigned)(rb * 16 + hp) * 8u, rw, 0u);
            }
        };
        if constexpr (FHP == 0) raise();
        const int t0 = tb >= 8 ? 0 : 8 - tb;
        const unsigned vstep = blk ? 0u : 1024u;
        const unsigned vb0 = ldsb + (blk ? (unsigned)(L_GW + (1 - h) * 128) : (unsigned)(L_VST + (nh * 12 + m) * 1024 + (4 * h + q) * 32 + p * 8));
        unsigned mk[16];
#pragma unroll
        for (int j = 0; j < 16; ++j) mk[j] = ((j & 3) + 8 * (j >> 2) + 4 * h <= r) ? 0xFFFFFFFFu : 0u;
        f32x16 zero;
#pragma unroll
        for (int j = 0; j < 16; ++j) zero[j] = 0.f;
        const float NEG = -INFINITY;
        auto score = [&](auto P, const f32x16& c) -> f32x16 { return MFMA32(kf[decltype(P)::v], qf, c); };
        auto merge = [&](const f32x16& a, const f32x16& b) {
            f32x16 o_;
#pragma unroll
            for (int j = 0; j < 16; ++j) { const float x_ = a[j], y_ = b[j]; o_[j] = __builtin_bit_cast(float, (__builtin_bit_cast(unsigned, x_) & mk[j]) | (__builtin_bit_cast(unsigned, y_) & ~mk[j])); }
            return o_;
        };
        float mx = NEG;
        auto red = [&](const f32x16& s_) {
#pragma unroll
            for (int j = 0; j < 16; j += 2) mx = fmaxf(fmaxf(s_[j], s_[j + 1]), mx);
        };
        {
            f32x16 S[4];
            S[0] = score(IC<0>{}, zero); S[1] = score(IC<1>{}, zero); S[2] = score(IC<2>{}, zero); S[3] = score(IC<3>{}, zero);
            SB();
            if constexpr (FHP == 1) { raise(); SB(); }
            red(merge(S[0], S[1]));
            S[0] = score(IC<4>{}, zero); S[1] = score(IC<5>{}, zero);
            SB();
            sfor<2, 9>([&](auto P) {
                constexpr int pp = decltype(P)::v;
                if constexpr (FHP == pp) { raise(); SB(); }
                if constexpr (FHL == pp) ld8c(fhv, voFH, rw, 0u);
                if (isC) { issueFp(pp - 2, 2 * ww, vW, vO); issueFp(pp - 2, 2 * ww + 1, vW, vO); }
                red(S[pp % 4]);
                if constexpr (pp + 4 < 9) S[pp % 4] = score(IC<pp + 4>{}, zero);
                SB();
            });
        }
        {
            const u32x2 sw = __builtin_amdgcn_permlane32_swap(__builtin_bit_cast(unsigned, mx), __builtin_bit_cast(unsigned, mx), false, false);
            const unsigned s0_ = sw[0], s1_ = sw[1];
            mx = fmaxf(__builtin_bit_cast(float, s0_), __builtin_bit_cast(float, s1_));
        }
        f32x16 negm;
#pragma unroll
        for (int j = 0; j < 16; ++j) negm[j] = -mx;
        FST(9);
        f32x16 S[3];
        S[0] = score(IC<0>{}, negm); S[1] = score(IC<1>{}, negm); S[2] = score(IC<2>{}, negm);
        bf16x8 vf[2][2];
        auto rdv = [&](auto P, bf16x8 (&v)[2]) {
            constexpr int pp = decltype(P)::v, tile = (pp + 8) % 9;
            const unsigned dz = tile < t0 ? (unsigned)(L_ZERO - L_GW) : 0u;
            const LAS char* p0 = lp(vb0 + vstep * (unsigned)tile + (blk ? dz : 0u));
            v[0] = cat8(trd(p0), trd(p0 + 256)); v[1] = cat8(trd(p0 + 512), trd(p0 + 768));
        };
        rdv(IC<0>{}, vf[0]); rdv(IC<1>{}, vf[1]);
        if constexpr (FHP == 9) raise();
        if constexpr (FHL == 9) ld8c(fhv, voFH, rw, 0u);
        FST(10);
        auto slot = [&](auto SL) {
            constexpr int sl = decltype(SL)::v;
            if constexpr (sl < 2) sfor<7 + 3 * sl, 10 + 3 * sl>([&](auto C) { issueFp(decltype(C)::v, w, vW, vO); });
            if constexpr (sl == FHL - 10) ld8c(fhv, voFH, rw, 0u);
            if constexpr (sl == FHS) {
                FST(11);
                waitv_tie2<(FHL >= 10 ? 3 * ((FHS < 1 ? FHS : 1) - (FHL - 10) > 0 ? (FHS < 1 ? FHS : 1) - (FHL - 10) : 0) : 3 * (FHS < 2 ? FHS + 1 : 2))>(fhv);
                unsigned spins = 0;
                while (!__all(fhv[0] == tok[0] && fhv[1] == tok[1]) && ++spins < SPIN_CAP) { __builtin_amdgcn_s_sleep(1); ld8c(fhv, voFH, rw, 0u); waitv_tie2<0>(fhv); }
                FST(12);
            }
            if constexpr (sl >= FHS) {
                constexpr int ns = 8 - FHS, c0 = 22 * (sl - FHS) / ns, c1 = 22 * (sl - FHS + 1) / ns;
                sfor<c0, c1>([&](auto C) { constexpr int c = decltype(C)::v; const int cc = ccF(c); ld16c(af[c], l16, rw, (cc == 21 && kq >= 2) ? WS_END : sAf + (unsigned)(4 * cc) * 1024u); });
            }
        };
        auto expack = [&](const f32x16& s_, u32x4& p0, u32x4& p1) {
#pragma unroll
            for (int j = 0; j < 4; ++j) {
                p0[j] = cvtpk(__builtin_amdgcn_exp2f(s_[2 * j]), __builtin_amdgcn_exp2f(s_[2 * j + 1]));
                p1[j] = cvtpk(__builtin_amdgcn_exp2f(s_[8 + 2 * j]), __builtin_amdgcn_exp2f(s_[8 + 2 * j + 1]));
            }
        };
        f32x16 o;
        SB();
        {
            u32x4 p0, p1;
            expack(merge(S[0], S[1]), p0, p1);
            S[0] = score(IC<3>{}, negm); S[1] = score(IC<4>{}, negm);
            u32x4 d0, d1, e0, e1;
#pragma unroll
            for (int j = 0; j < 4; ++j) {
                const unsigned m0 = (mk[2 * j] & 0xFFFFu) | (mk[2 * j + 1] & 0xFFFF0000u), m1 = (mk[8 + 2 * j] & 0xFFFFu) | (mk[8 + 2 * j + 1] & 0xFFFF0000u);
                const unsigned a0 = p0[j], a1 = p1[j];
                d0[j] = a0 & m0; e0[j] = a0 & ~m0; d1[j] = a1 & m1; e1[j] = a1 & ~m1;
            }
            o = MFMA32(vf[0][0], __builtin_bit_cast(bf16x8, d0), zero);
            o = MFMA32(vf[0][1], __builtin_bit_cast(bf16x8, d1), o);
            rdv(IC<2>{}, vf[0]);
            o = MFMA32(vf[1][0], __builtin_bit_cast(bf16x8, e0), o);
            o = MFMA32(vf[1][1], __builtin_bit_cast(bf16x8, e1), o);
            slot(IC<0>{});
            SB();
        }
        sfor<2, 9>([&](auto P) {
            constexpr int pp = decltype(P)::v;
            if constexpr (pp + 1 < 9) rdv(IC<pp + 1>{}, vf[(pp + 1) & 1]);
            u32x4 p0, p1;
            expack(S[pp % 3], p0, p1);
            if constexpr (pp + 3 < 9) S[pp % 3] = score(IC<pp + 3>{}, negm);
            o = MFMA32(vf[pp & 1][0], __builtin_bit_cast(bf16x8, p0), o);
            o = MFMA32(vf[pp & 1][1], __builtin_bit_cast(bf16x8, p1), o);
            slot(IC<pp - 1>{});
            SB();
        });
        const float sc = g / o[8];
        st16c(nosent(pack8(o, 0, sc)), l16, rw, WS_A + (unsigned)((tb * NKS + NKS_H + hp * 2 + nh) * 1024));
    }
    STAMP(5);

    {
        const bool boss = blockIdx.x == 0 && w == 0;
        u32x4 ea[2];
        unsigned woff[2];
#pragma unroll
        for (int t = 0; t < 2; ++t) { const int row = 16 * kq + 4 * h + q; const int c16 = (t * 4 + blk * 2 + (p >> 1)) ^ (((row >> 1) & 1) << 2); woff[t] = (unsigned)(row * 128 + c16 * 16 + (p & 1) * 8); }
        f32x16 acc[2];
#pragma unroll
        for (int i = 0; i < 2; ++i)
#pragma unroll
            for (int j = 0; j < 16; ++j) acc[i][j] = 0.f;
        auto mm = [&](auto C) {
            constexpr int c = decltype(C)::v;
            const LAS char* wb = L + fsl(c);
#pragma unroll
            for (int t = 0; t < 2; ++t) {
                const bf16x8 wf = cat8(trd(wb + woff[t]), trd(wb + woff[t] + 8 * 128));
                acc[t] = MFMA32(wf, __builtin_bit_cast(bf16x8, af[c]), acc[t]);
            }
        };
        auto issueF1 = [&](auto C) { issueFp(decltype(C)::v, w, vW, vO); };
        waitv_bar<(FHS == 0 ? 21 : 23)>();
        STAMP(6);
        if (boss) { ld16c(ea[0], l16, rw, WS_FH); ld16c(ea[1], l16, rw, WS_FH + 1024u); }
        auto ldO = [&](auto C) { constexpr int c = decltype(C)::v; ld16c(af[c], l16, rw, sAf + (unsigned)(NKS_H + 4 * (c - 22)) * 1024u); };
        sfor<0, 6>([&](auto C) { constexpr int c = decltype(C)::v; waitv_tie<22>(af[c]); mm(C); issueF1(IC<13 + c>{}); SB(); });
        FST(29);
        BAR_L();
        FST(30);
        sfor<6, 13>([&](auto C) { constexpr int c = decltype(C)::v; waitv_tie<22>(af[c]); mm(C); if constexpr (c < 12) issueF1(IC<13 + c>{}); else ldO(IC<22>{}); SB(); });
        STAMP(7);
        waitv_bar<7>();
        FST(33);
        sfor<13, 22>([&](auto C) { TIE(af[decltype(C)::v]); });
        bool epoch_done = !boss;
        if (boss) {
            TIE(ea[0]); TIE(ea[1]);
            bool ok = true;
#pragma unroll
            for (int j = 0; j < 2; ++j) { const unsigned a0 = ea[j][0], a1 = ea[j][1], a2 = ea[j][2], a3 = ea[j][3]; ok = ok && a0 == tok[0] && a1 == tok[1] && a2 == tok[0] && a3 == tok[1]; }
            if (__all(ok)) { epoch_done = true; if (lane == 0) { const unsigned long long e1 = epoch + 1; const u32x2 ev1 = {(unsigned)e1, (unsigned)(e1 >> 32)}; st8c(ev1, 0u, rw, WS_E); } }
        }
        sfor<13, 19>([&](auto C) {
            constexpr int c = decltype(C)::v;
            mm(C);
            if constexpr (c < 18) { issueF1(IC<12 + c>{}); ldO(IC<10 + c>{}); } else { ldO(IC<28>{}); ldO(IC<29>{}); }
            SB();
        });
        FST(35);
        waitv_bar<13>();
        FST(36);
        sfor<19, 22>(mm);
        STAMP(8);
        auto validO = [&](auto C0) {
            constexpr int c0 = decltype(C0)::v, c1 = c0 == 22 ? 25 : 30;
            unsigned mxs = 0;
            sfor<c0, c1>([&](auto C) { TIE(af[decltype(C)::v]); mxs = max(mxs, anysent(af[decltype(C)::v])); });
            if (__builtin_expect(!__all(mxs != SENT), 0)) {
                unsigned spins = 0;
                for (;;) {
                    __builtin_amdgcn_s_sleep(1);
                    sfor<c0, 30>(ldO);
                    waitv<0>();
                    mxs = 0;
                    sfor<c0, 30>([&](auto C) { TIE(af[decltype(C)::v]); mxs = max(mxs, anysent(af[decltype(C)::v])); });
                    if (__all(mxs != SENT) || ++spins >= SPIN_CAP) break;
                }
            }
        };
        waitv<8>();
        validO(IC<22>{});
        sfor<22, 25>(mm);
        waitv<0>();
        validO(IC<25>{});
        STAMP(9);
        BAR_L();
        LAS char* dst = L + kq * (64 * RP) + (fm * 32 + r) * RP + 16 * h;
        auto mt = [&](auto C, auto T) {
            constexpr int c = decltype(C)::v, t = decltype(T)::v;
            const LAS char* wb = L + fsl(c);
            const bf16x8 wf = cat8(trd(wb + woff[t]), trd(wb + woff[t] + 8 * 128));
            acc[t] = MFMA32(wf, __builtin_bit_cast(bf16x8, af[c]), acc[t]);
        };
        auto wrp = [&](int t, int g4) { *(LAS f32x4*)(dst + (t ? RB1 : RB0) + g4 * 32) = (f32x4){acc[t][4 * g4], acc[t][4 * g4 + 1], acc[t][4 * g4 + 2], acc[t][4 * g4 + 3]}; };
        sfor<25, 30>([&](auto C) { mt(C, IC<0>{}); });
        SB();
        sfor<25, 30>([&](auto C) { constexpr int c = decltype(C)::v; mt(C, IC<1>{}); if constexpr (c > 25) wrp(0, c - 26); SB(); });
        STAMP(10);
#pragma unroll
        for (int g4 = 0; g4 < 4; ++g4) wrp(1, g4);
        BAR_L();
        {
            f32x4 a = {0.f, 0.f, 0.f, 0.f}, b = a;
#pragma unroll
            for (int k = 0; k < 4; ++k) {
                const LAS char* src = lp(rsrc_) + k * (64 * RP);
                a += *(const LAS f32x4*)src; b += *(const LAS f32x4*)(src + 16);
            }
            u32x4 o = {cvtpk(a[0], a[1]), cvtpk(a[2], a[3]), cvtpk(b[0], b[1]), cvtpk(b[2], b[3])};
            st16c(o, voOut, mkrs(out, S_ * D_ * 2), 0u);
        }
        if (!epoch_done) {
            unsigned spins = 0;
            for (;;) {
                ld16c(ea[0], l16, rw, WS_FH); ld16c(ea[1], l16, rw, WS_FH + 1024u); waitv<0>(); TIE(ea[0]); TIE(ea[1]);
                bool ok = true;
#pragma unroll
                for (int j = 0; j < 2; ++j) { const unsigned a0 = ea[j][0], a1 = ea[j][1], a2 = ea[j][2], a3 = ea[j][3]; ok = ok && a0 == tok[0] && a1 == tok[1] && a2 == tok[0] && a3 == tok[1]; }
                if (__all(ok) || ++spins >= SPIN_CAP) break;
                __builtin_amdgcn_s_sleep(2);
            }
            if (lane == 0) { const unsigned long long e1 = epoch + 1; const u32x2 ev1 = {(unsigned)e1, (unsigned)(e1 >> 32)}; st8c(ev1, 0u, rw, WS_E); }
        }
    }
    asm volatile(".Lpf_end:");
}

extern "C" void kernel_launch(void* const* d_in, const int* in_sizes, int n_in, void* d_out, int out_size, void* d_ws, size_t ws_size, hipStream_t stream) {
    (void)hipFuncSetAttribute((const void*)k_fused, hipFuncAttributeMaxDynamicSharedMemorySize, LDS_BYTES);
    k_fused<<<256, 512, LDS_BYTES, stream>>>(d_in[0], d_in[1], d_in[2], d_in[4], d_in[5], d_in[6], (char*)d_ws, d_out);
}
```
